```python
import torch

def _softmax(x, axis=-1):
    return torch.softmax(x, dim=axis)
def _gelu(x):
    return torch.nn.functional.gelu(x, approximate="tanh")
def _silu(x):
    return torch.nn.functional.silu(x)
def _sigmoid(x):
    return torch.sigmoid(x)
def _layernorm(x, axis=-1, eps=1e-5):
    m = x.mean(dim=axis, keepdim=True)
    v = x.var(dim=axis, keepdim=True, unbiased=False)
    return (x - m) / torch.sqrt(v + eps)
def _scatter_add(tgt, idx, src, axis=0):
    return tgt.clone().index_add_(axis, idx, src)
def _segment_sum(src, seg, n):
    out = torch.zeros((n,) + src.shape[1:], dtype=src.dtype)
    return out.index_add_(0, seg, src)
def _pad(x, pw):
    flat = tuple(v for pair in reversed(pw) for v in pair)
    return torch.nn.functional.pad(x, flat)

kernel_name = 'interleave_layers__sliding_block_cached__glu_gated_glu__decode'

def setup_inputs():

    _w = (lambda a: (lambda t: t.to(torch.bfloat16) if t.is_floating_point() else t)(torch.as_tensor(a)))
    def _A_setup():
        B, Sq, D = 1, 1, 512
        Hq, Hkv, Sk, Dh = 32, 8, 32768, 128
        assert Hq % Hkv == 0, (Hq, Hkv)
        return {
            "x": _w((lambda *s: torch.randn(*s, dtype=torch.bfloat16) * 0.05)(B, Sq, D)),
            "k_cache": _w((lambda *s: torch.randn(*s, dtype=torch.bfloat16) * 1.6)(B, Hkv, Sk, Dh)),
            "v_cache": _w((lambda *s: torch.randn(*s, dtype=torch.bfloat16) * 1.6)(B, Hkv, Sk, Dh)),
            "wq": _w((lambda *s: torch.randn(*s, dtype=torch.bfloat16) * 1.6)(D, Hq * Dh) * (D ** -0.5)),
            "wo": _w((lambda *s: torch.randn(*s, dtype=torch.bfloat16) * 1.6)(Hq * Dh, D) * ((Hq * Dh) ** -0.5)),
        }
    _A_in = _A_setup()

    def _B_setup():
        B, S, D, F = 1, 1, 512, 1792
        return {
            "x": _w((lambda *s: torch.randn(*s, dtype=torch.bfloat16) * 0.05)(B, S, D)),
            "w_gate": _w((lambda *s: torch.randn(*s, dtype=torch.bfloat16) * 0.05)(D, F)),
            "w_up": _w((lambda *s: torch.randn(*s, dtype=torch.bfloat16) * 0.05)(D, F)),
            "w_down": _w((lambda *s: torch.randn(*s, dtype=torch.bfloat16) * 0.05)(F, D)),
        }
    _B_in = _B_setup()

    return {
        "A_x": _A_in["x"],
        "A_k_cache": _A_in["k_cache"],
        "A_v_cache": _A_in["v_cache"],
        "A_wq": _A_in["wq"],
        "A_wo": _A_in["wo"],
        "B_x": _B_in["x"],
        "B_w_gate": _B_in["w_gate"],
        "B_w_up": _B_in["w_up"],
        "B_w_down": _B_in["w_down"],
    }

def reference(A_x, A_k_cache, A_v_cache, A_wq, A_wo, B_x, B_w_gate, B_w_up, B_w_down):
    def _A_ref(x, k_cache, v_cache, wq, wo):
        B, Sq, D = x.shape
        Hq, Dh = 32, 128
        Hkv, Sk = k_cache.shape[1], k_cache.shape[2]
        W = min(4096, max(256, Sk // 4))
        rep = Hq // Hkv
        q = (lambda x, *d: x.permute(*d))((x @ wq).reshape(B, Sq, Hq, Dh), 0, 2, 1, 3)
        k = (lambda x, n, axis: torch.repeat_interleave(x, n, dim=axis))(k_cache[:, :, Sk - W:, :], rep, axis=1)
        v = (lambda x, n, axis: torch.repeat_interleave(x, n, dim=axis))(v_cache[:, :, Sk - W:, :], rep, axis=1)
        scores = torch.einsum("bhqd,bhkd->bhqk", q, k) * (Dh ** -0.5)
        p = _softmax(scores, axis=-1)
        o = torch.einsum("bhqk,bhkd->bhqd", p, v)
        return (lambda x, *d: x.permute(*d))(o, 0, 2, 1, 3).reshape(B, Sq, Hq * Dh) @ wo

    def _B_ref(x, w_gate, w_up, w_down):
        g = torch.einsum("bsd,df->bsf", x, w_gate)
        u = torch.einsum("bsd,df->bsf", x, w_up)
        h = _sigmoid(g) * u
        return torch.einsum("bsf,fd->bsd", h, w_down)

    x = A_x
    x = x + _A_ref(_layernorm(x, axis=-1), A_k_cache, A_v_cache, A_wq, A_wo)
    x = x + _B_ref(_layernorm(x, axis=-1), B_w_gate, B_w_up, B_w_down)
    x = x + _A_ref(_layernorm(x, axis=-1), A_k_cache, A_v_cache, A_wq, A_wo)
    x = x + _B_ref(_layernorm(x, axis=-1), B_w_gate, B_w_up, B_w_down)
    return x
```

```cpp
#include <hip/hip_runtime.h>
#include <hip/hip_bf16.h>
#include <stdint.h>

typedef unsigned short u16;
__device__ __forceinline__ float bf2f(u16 b) { return __uint_as_float(((unsigned)b) << 16); }
__device__ __forceinline__ float rbf(float f) {
  return f; }
__device__ __forceinline__ u16 f2bf(float f) { unsigned u = __float_as_uint(f); u += 0x7fffu + ((u >> 16) & 1u); return (u16)(u >> 16); }

constexpr int D = 512, HQ = 32, HKV = 8, SK = 32768, DH = 128, W = 4096, F = 1792;

__device__ void layernorm_block(const float* x, float* xn, float* red) {
  int t = threadIdx.x;
  float a = x[t], b = x[t + 256];
  float s = a + b;
  for (int o = 32; o >= 1; o >>= 1) s += __shfl_xor(s, o);
  if ((t & 63) == 0) red[t >> 6] = s;
  __syncthreads();
  float mean = (red[0] + red[1] + red[2] + red[3]) * (1.f / D);
  __syncthreads();
  float da = a - mean, db = b - mean;
  float v = da * da + db * db;
  for (int o = 32; o >= 1; o >>= 1) v += __shfl_xor(v, o);
  if ((t & 63) == 0) red[t >> 6] = v;
  __syncthreads();
  float var = (red[0] + red[1] + red[2] + red[3]) * (1.f / D);
  float mb = rbf(mean), vb = rbf(var);
  float sd = rbf(sqrtf(rbf(vb + 1e-5f)));
  xn[t] = rbf(rbf(a - mb) / sd);
  xn[t + 256] = rbf(rbf(b - mb) / sd);
  __syncthreads();
}

__global__ void k_init(const u16* x, float* xf) { int t = threadIdx.x; xf[t] = bf2f(x[t]); xf[t + 256] = bf2f(x[t + 256]); }

__global__ void __launch_bounds__(256) k_q(const float* x, const u16* wq, float* q) {
  __shared__ float xn[D]; __shared__ float red[4]; __shared__ float acc[32][64];
  layernorm_block(x, xn, red);
  int t = threadIdx.x, c = t & 7, r = t >> 3;
  float a[8] = {0};
  for (int k = r; k < D; k += 32) {
    uint4 w = *(const uint4*)(wq + (size_t)k * (HQ * DH) + blockIdx.x * 64 + c * 8);
    float xv = xn[k];
    unsigned ww[4] = {w.x, w.y, w.z, w.w};
    for (int j = 0; j < 4; ++j) { a[2 * j] += xv * __uint_as_float(ww[j] << 16); a[2 * j + 1] += xv * __uint_as_float(ww[j] & 0xffff0000u); }
  }
  for (int j = 0; j < 8; ++j) acc[r][c * 8 + j] = a[j];
  __syncthreads();
  if (t < 64) { float s = 0; for (int i = 0; i < 32; ++i) s += acc[i][t]; q[blockIdx.x * 64 + t] = rbf(s); }
}

__global__ void __launch_bounds__(256) k_attn(const float* q, const u16* kc, const u16* vc, float* part) {
  __shared__ float qs[4][DH]; __shared__ float sc[4][128]; __shared__ float mx[4], sm[4];
  int g = blockIdx.x & 7, s = blockIdx.x >> 3, t = threadIdx.x;
  qs[t >> 7][t & 127] = q[g * 512 + t]; qs[2 + (t >> 7)][t & 127] = q[g * 512 + 256 + t];
  __syncthreads();
  const u16* kb = kc + ((size_t)g * SK + (SK - W) + s * 128) * DH;
  const u16* vb = vc + ((size_t)g * SK + (SK - W) + s * 128) * DH;
  {
    int key = t >> 1, hf = t & 1;
    float a[4] = {0, 0, 0, 0};
    for (int c = 0; c < 8; ++c) {
      uint4 w = *(const uint4*)(kb + key * DH + hf * 64 + c * 8);
      unsigned ww[4] = {w.x, w.y, w.z, w.w};
      for (int j = 0; j < 4; ++j) { float lo = __uint_as_float(ww[j] << 16), hi = __uint_as_float(ww[j] & 0xffff0000u);
        for (int h = 0; h < 4; ++h) a[h] += lo * qs[h][hf * 64 + c * 8 + 2 * j] + hi * qs[h][hf * 64 + c * 8 + 2 * j + 1]; }
    }
    for (int h = 0; h < 4; ++h) { a[h] += __shfl_xor(a[h], 1); if (hf == 0) sc[h][key] = rbf(rbf(a[h]) * 0.08838834764831845f); }
  }
  __syncthreads();
  { int h = t >> 6, l = t & 63; float a = sc[h][l], b = sc[h][l + 64]; float m = fmaxf(a, b);
    for (int o = 32; o >= 1; o >>= 1) m = fmaxf(m, __shfl_xor(m, o));
    a = __expf(a - m); b = __expf(b - m); float ss = a + b;
    for (int o = 32; o >= 1; o >>= 1) ss += __shfl_xor(ss, o);
    sc[h][l] = a; sc[h][l + 64] = b; if (l == 0) { mx[h] = m; sm[h] = ss; } }
  __syncthreads();
  { int d = t & 127, hp = t >> 7; float a0 = 0, a1 = 0;
    for (int k = 0; k < 128; ++k) { float v = bf2f(vb[k * DH + d]); a0 += sc[2 * hp][k] * v; a1 += sc[2 * hp + 1][k] * v; }
    float* p0 = part + ((size_t)(g * 4 + 2 * hp) * 32 + s) * 130; float* p1 = part + ((size_t)(g * 4 + 2 * hp + 1) * 32 + s) * 130;
    p0[d] = a0; p1[d] = a1;
    if (d == 0) { p0[128] = mx[2 * hp]; p0[129] = sm[2 * hp]; p1[128] = mx[2 * hp + 1]; p1[129] = sm[2 * hp + 1]; } }
}

__global__ void __launch_bounds__(128) k_comb(const float* part, float* o) {
  int h = blockIdx.x, d = threadIdx.x; const float* p = part + (size_t)h * 32 * 130;
  float M = -INFINITY; for (int s = 0; s < 32; ++s) M = fmaxf(M, p[s * 130 + 128]);
  float L = 0, a = 0; for (int s = 0; s < 32; ++s) { float w = __expf(p[s * 130 + 128] - M); L += w * p[s * 130 + 129]; a += w * p[s * 130 + d]; }
  o[h * DH + d] = rbf(a / L);
}

template <int K>
__global__ void __launch_bounds__(256) k_proj(const float* in, const u16* w, const float* x, float* xo, u16* out) {
  __shared__ float acc[128][16];
  int t = threadIdx.x, c = t & 1, r = t >> 1;
  float a[8] = {0};
  for (int k = r; k < K; k += 128) {
    uint4 ww4 = *(const uint4*)(w + (size_t)k * D + blockIdx.x * 16 + c * 8);
    float xv = in[k]; unsigned ww[4] = {ww4.x, ww4.y, ww4.z, ww4.w};
    for (int j = 0; j < 4; ++j) { a[2 * j] += xv * __uint_as_float(ww[j] << 16); a[2 * j + 1] += xv * __uint_as_float(ww[j] & 0xffff0000u); }
  }
  for (int j = 0; j < 8; ++j) acc[r][c * 8 + j] = a[j];
  __syncthreads();
  if (t < 16) { float s = 0; for (int i = 0; i < 128; ++i) s += acc[i][t]; int col = blockIdx.x * 16 + t; float r2 = rbf(x[col] + rbf(s)); xo[col] = r2; if (out) out[col] = f2bf(r2); }
}

__global__ void __launch_bounds__(256) k_gu(const float* x, const u16* wg, const u16* wu, float* h) {
  __shared__ float xn[D]; __shared__ float red[4]; __shared__ float acc[32][64];
  layernorm_block(x, xn, red);
  int t = threadIdx.x, c = t & 7, r = t >> 3;
  const u16* w = (c < 4) ? wg : wu; int cc = c & 3;
  float a[8] = {0};
  for (int k = r; k < D; k += 32) {
    uint4 w4 = *(const uint4*)(w + (size_t)k * F + blockIdx.x * 32 + cc * 8);
    float xv = xn[k]; unsigned ww[4] = {w4.x, w4.y, w4.z, w4.w};
    for (int j = 0; j < 4; ++j) { a[2 * j] += xv * __uint_as_float(ww[j] << 16); a[2 * j + 1] += xv * __uint_as_float(ww[j] & 0xffff0000u); }
  }
  for (int j = 0; j < 8; ++j) acc[r][c * 8 + j] = a[j];
  __syncthreads();
  if (t < 32) { float g = 0, u = 0; for (int i = 0; i < 32; ++i) { g += acc[i][t]; u += acc[i][32 + t]; }
    g = rbf(g); u = rbf(u); float sg = rbf(1.f / (1.f + __expf(-g))); h[blockIdx.x * 32 + t] = rbf(sg * u); }
}

extern "C" void kernel_launch(void* const* d_in, const int* in_sizes, int n_in, void* d_out, int out_size, void* d_ws, size_t ws_size, hipStream_t stream) {
  const u16* x = (const u16*)d_in[0]; const u16* kc = (const u16*)d_in[1]; const u16* vc = (const u16*)d_in[2];
  const u16* wq = (const u16*)d_in[3]; const u16* wo = (const u16*)d_in[4];
  const u16* wg = (const u16*)d_in[6]; const u16* wu = (const u16*)d_in[7]; const u16* wd = (const u16*)d_in[8];
  float* ws = (float*)d_ws; float* x0 = ws; float* x1 = ws + 512; float* q = ws + 1024; float* o = q + 4096; float* h = o + 4096; float* part = h + 2048;
  k_init<<<1, 256, 0, stream>>>(x, x0);
  for (int l = 0; l < 2; ++l) {
    k_q<<<64, 256, 0, stream>>>(x0, wq, q);
    k_attn<<<256, 256, 0, stream>>>(q, kc, vc, part);
    k_comb<<<32, 128, 0, stream>>>(part, o);
    k_proj<4096><<<32, 256, 0, stream>>>(o, wo, x0, x1, nullptr);
    k_gu<<<56, 256, 0, stream>>>(x1, wg, wu, h);
    k_proj<1792><<<32, 256, 0, stream>>>(h, wd, x1, x0, l == 1 ? (u16*)d_out : nullptr);
  }
}
```

```cpp
#include <hip/hip_runtime.h>
#include <stdint.h>

typedef unsigned short u16;
typedef unsigned v4u __attribute__((ext_vector_type(4)));
typedef unsigned v2u __attribute__((ext_vector_type(2)));
typedef float f32x4 __attribute__((ext_vector_type(4)));
typedef float f32x2 __attribute__((ext_vector_type(2)));
typedef short s16x4 __attribute__((ext_vector_type(4)));
typedef __bf16 bf16x8 __attribute__((ext_vector_type(8)));
typedef __bf16 bf16x2 __attribute__((ext_vector_type(2)));
#define LAS __attribute__((address_space(3)))
#define GAS __attribute__((address_space(1)))

constexpr int SK = 32768, WIN = 4096, DH = 128, DM = 512, FF = 1792;
constexpr int WS_Q = 4096, WS_Y = 40960, WS_H = 73728, WS_X = 90112, WS_O = 131072, WS_BYTES = 1 << 20;
constexpr int WS_STAMP = 4 << 20;
constexpr int L_STG = 0, STG_W = 7168, L_ATT = 8 * STG_W, L_ML = L_ATT + 16384, L_RED = L_ML + 256, RED_SZ = 640;
constexpr int LDS_BYTES = 84 * 1024;
static_assert(L_RED + 8 * RED_SZ <= LDS_BYTES, "lds");
constexpr int TW = 7;
constexpr unsigned SPIN_CAP = 1u << 17;

#define D_Q 4
#define D_O 2
#define D_Y 9
#define D_H 14
#define D_XG 40
#define D_XD 9

#define WO_AT 1
#define FF_AT 1
#define SCHED() __builtin_amdgcn_sched_barrier(0)
#define STAMP(i) do {} while (0)
#define PASS(i) do {} while (0)
#define STAMP_FLUSH() do {} while (0)
#define BACKOFF 2
#define CODE_PF 1

__device__ __forceinline__ float bflo(unsigned u) { return __uint_as_float(u << 16); }
__device__ __forceinline__ float bfhi(unsigned u) { return __uint_as_float(u & 0xffff0000u); }
__device__ __forceinline__ unsigned pk(float lo, float hi) { bf16x2 p = {(__bf16)lo, (__bf16)hi}; return __builtin_bit_cast(unsigned, p); }
__device__ __forceinline__ float rbf(float f) { return bflo((unsigned)__builtin_bit_cast(u16, (__bf16)f)); }
template <int CTRL> __device__ __forceinline__ float dpp(float x) {
  return __builtin_bit_cast(float, __builtin_amdgcn_mov_dpp(__builtin_bit_cast(int, x), CTRL, 0xf, 0xf, true));
}
template <int CTRL> __device__ __forceinline__ unsigned dppu(unsigned x) { return (unsigned)__builtin_amdgcn_mov_dpp((int)x, CTRL, 0xf, 0xf, true); }
constexpr int XOR1 = 0xB1, XOR2 = 0x4E, HMIR = 0x141, MIR = 0x140, ROR4 = 0x124, ROR8 = 0x128;
__device__ __forceinline__ float row_sum(float v) { v += dpp<XOR1>(v); v += dpp<XOR2>(v); v += dpp<HMIR>(v); v += dpp<MIR>(v); return v; }
__device__ __forceinline__ float row_max(float v) { v = fmaxf(v, dpp<XOR1>(v)); v = fmaxf(v, dpp<XOR2>(v)); v = fmaxf(v, dpp<HMIR>(v)); v = fmaxf(v, dpp<MIR>(v)); return v; }
__device__ __forceinline__ float xrow_sum(float x) {
  auto s = __builtin_amdgcn_permlane16_swap(__float_as_uint(x), __float_as_uint(x), false, false);
  x = __uint_as_float(s[0]) + __uint_as_float(s[1]);
  auto t = __builtin_amdgcn_permlane32_swap(__float_as_uint(x), __float_as_uint(x), false, false);
  return __uint_as_float(t[0]) + __uint_as_float(t[1]);
}
__device__ __forceinline__ float xrow_max(float x) {
  auto s = __builtin_amdgcn_permlane16_swap(__float_as_uint(x), __float_as_uint(x), false, false);
  x = fmaxf(__uint_as_float(s[0]), __uint_as_float(s[1]));
  auto t = __builtin_amdgcn_permlane32_swap(__float_as_uint(x), __float_as_uint(x), false, false);
  return fmaxf(__uint_as_float(t[0]), __uint_as_float(t[1]));
}
__device__ __forceinline__ f32x4 mfma32(v4u a, v4u b, f32x4 c) {
  return __builtin_amdgcn_mfma_f32_16x16x32_bf16(__builtin_bit_cast(bf16x8, a), __builtin_bit_cast(bf16x8, b), c, 0, 0, 0);
}
__device__ __forceinline__ f32x4 mfma16(v2u a, v2u b, f32x4 c) {
  return __builtin_amdgcn_mfma_f32_16x16x16bf16_1k(__builtin_bit_cast(s16x4, a), __builtin_bit_cast(s16x4, b), c, 0, 0, 0);
}
__device__ __forceinline__ v2u trread(LAS unsigned char* p) { return __builtin_bit_cast(v2u, __builtin_amdgcn_ds_read_tr16_b64_v4i16((LAS s16x4*)p)); }
__device__ __forceinline__ v4u ldg(const u16* p) { return *(const GAS v4u*)p; }
__device__ __forceinline__ v4u ldg_nt(const u16* p) { return __builtin_nontemporal_load((const GAS v4u*)p); }
__device__ __forceinline__ v4u ld_sc1(__amdgpu_buffer_rsrc_t rs, int off) { return __builtin_amdgcn_raw_buffer_load_b128(rs, off, 0, 16); }
__device__ __forceinline__ void st_sc1(__amdgpu_buffer_rsrc_t rs, int off, v4u v) { __builtin_amdgcn_raw_buffer_store_b128(v, rs, off, 0, 16); }
#define PERM(hi, lo, sel) __builtin_amdgcn_perm((hi), (lo), (sel))
__device__ __forceinline__ bool okH(v4u d, unsigned T) { return (PERM(d.y, d.x, 0x05040100u) == T) & (PERM(d.w, d.z, 0x05040100u) == T); }
__device__ __forceinline__ v2u valH(v4u d) { return v2u{PERM(d.y, d.x, 0x07060302u), PERM(d.w, d.z, 0x07060302u)}; }
__device__ __forceinline__ v4u mkH(float a, float b, float c, float d, unsigned T) {
  const unsigned p0 = pk(a, b), p1 = pk(c, d);
  return v4u{PERM(p0, T, 0x05040100u), PERM(p0, T, 0x07060302u), PERM(p1, T, 0x05040100u), PERM(p1, T, 0x07060302u)};
}
__device__ __forceinline__ bool okF(v4u d, unsigned T) {
  const unsigned a = PERM(d.y, d.x, 0x0c0c0400u), b = PERM(d.w, d.z, 0x04000c0cu);
  return (a | b) == T;
}
__device__ __forceinline__ v4u mkF(f32x4 v, unsigned T) {
  return v4u{PERM(__float_as_uint(v[0]), T, 0x07060500u), PERM(__float_as_uint(v[1]), T, 0x07060501u),
             PERM(__float_as_uint(v[2]), T, 0x07060502u), PERM(__float_as_uint(v[3]), T, 0x07060503u)};
}
#define BARRIER() do { asm volatile("s_waitcnt lgkmcnt(0)" ::: "memory"); __builtin_amdgcn_s_barrier(); asm volatile("" ::: "memory"); } while (0)
#define SLEEP(n) do { if ((n) > 0) __builtin_amdgcn_s_sleep(n); } while (0)
#define DELAY(i, n) SLEEP(n)

__device__ __forceinline__ float gemv64(v2u a, const v2u (&B)[4]) {
  const f32x4 z = {0.f, 0.f, 0.f, 0.f};
  const f32x4 c0 = mfma16(a, B[0], z), c1 = mfma16(a, B[1], z), c2 = mfma16(a, B[2], z), c3 = mfma16(a, B[3], z);
  return (c0[0] + c1[1]) + (c2[2] + c3[3]);
}
__device__ __forceinline__ float colsum64(const v2u (&B)[4]) {
  const v2u one = {0x3f803f80u, 0x3f803f80u};
  f32x4 c = {0.f, 0.f, 0.f, 0.f};
  c = mfma16(one, B[0], c); c = mfma16(one, B[1], c); c = mfma16(one, B[2], c); c = mfma16(one, B[3], c);
  return c[0];
}
__device__ __forceinline__ f32x4 tail_sum(LAS unsigned char* red, int lane) {
  const int w2 = (lane >> 2) & 3, q = lane & 3;
  f32x4 v = *(LAS f32x4*)(red + w2 * 80 + q * 16) + *(LAS f32x4*)(red + (w2 + 4) * 80 + q * 16);
#pragma unroll
  for (int k = 0; k < 4; ++k) { float t = v[k]; t += dpp<ROR8>(t); t += dpp<ROR4>(t); v[k] = t; }
  return v;
}
__device__ __forceinline__ f32x2 tail_stats(LAS unsigned char* red, int lane) {
  const int w2 = (lane >> 2) & 3;
  f32x2 s = *(LAS f32x2*)(red + w2 * 80 + 64) + *(LAS f32x2*)(red + (w2 + 4) * 80 + 64);
#pragma unroll
  for (int k = 0; k < 2; ++k) { float t = s[k]; t += dpp<ROR8>(t); t += dpp<ROR4>(t); s[k] = t; }
  return s;
}
__device__ __forceinline__ f32x2 wave_stats(f32x4 x) {
  float s1 = (x[0] + x[1]) + (x[2] + x[3]);
  float s2 = fmaf(x[0], x[0], x[1] * x[1]) + fmaf(x[2], x[2], x[3] * x[3]);
  s1 += dpp<XOR1>(s1); s2 += dpp<XOR1>(s2); s1 += dpp<XOR2>(s1); s2 += dpp<XOR2>(s2);
  return f32x2{xrow_sum(s1), xrow_sum(s2)};
}

struct Ctx {
  const u16 *wo, *wg, *wu, *wd; u16* out; unsigned char* ws;
  __amdgpu_buffer_rsrc_t rs; LAS unsigned char* L; LAS unsigned char* stg;
  int lane, w, kg, i16, b, g, c, U, dcol; bool is_gu; unsigned E, T;
};

__device__ __forceinline__ void load_wo(const Ctx& X, v4u (&Aw)[6]) {
#pragma unroll
  for (int n = 0; n < 4; ++n) Aw[n] = ldg(X.wo + (size_t)(512 * X.g + 128 * (X.w >> 1) + 32 * n + (X.lane >> 1)) * DM + 16 * X.c + 8 * (X.lane & 1));
  SCHED();
}
template <bool GU> __device__ __forceinline__ void load_ff(const Ctx& X, v4u (&Bd)[7]) {
  if (GU) {
    const u16* wsel = (X.lane & 1) ? X.wu : X.wg;
#pragma unroll
    for (int n = 0; n < 2; ++n) Bd[n] = ldg(wsel + (size_t)(64 * X.w + 32 * n + (X.lane >> 1)) * FF + 8 * (28 * X.g + X.c));
  } else {
#pragma unroll
    for (int n = 0; n < 7; ++n) Bd[n] = ldg(X.wd + (size_t)(224 * X.w + 32 * n + (X.lane >> 1)) * DM + 16 * X.dcol + 8 * (X.lane & 1));
  }
  SCHED();
}

template <int layer, bool GU>
__device__ __forceinline__ void run_layer(Ctx& X, const v4u (&Kf)[4], v4u (&Vf)[4], const v2u (&Bq)[4], v4u (&Aw)[6], v4u (&Bd)[7],
                                          f32x4& xres, const f32x4& csq, f32x4& csgu, f32x4& xd) {
  const int lane = X.lane, w = X.w, kg = X.kg, i16 = X.i16, g = X.g, c = X.c, b = X.b;
  const unsigned T = X.T; const __amdgpu_buffer_rsrc_t rs = X.rs; LAS unsigned char* L = X.L; LAS unsigned char* stg = X.stg; unsigned char* ws = X.ws;
  (void)b; (void)ws;
  LAS unsigned char* red_wo = L + L_RED + (3 + 3 * layer) * RED_SZ;
  LAS unsigned char* red_ff = L + L_RED + (4 + 3 * layer) * RED_SZ;
  {
    DELAY(0 + 8 * layer, D_Q);
    v4u qf[4];
    {
      const int qoff = WS_Q + layer * 16384 + g * 2048 + ((i16 >> 2) * 128 + 8 * kg) * 4;
      v4u d[8];
#define QLOADS() _Pragma("unroll") for (int k = 0; k < 8; ++k) d[k] = ld_sc1(rs, qoff + (k >> 1) * 128 + (k & 1) * 16)
#define QOK() (okH(d[0], T) & okH(d[1], T) & okH(d[2], T) & okH(d[3], T) & okH(d[4], T) & okH(d[5], T) & okH(d[6], T) & okH(d[7], T))
      QLOADS(); SCHED();
      if (layer == 0 && WO_AT == 1) load_wo(X, Aw);
      if (layer == 0 && FF_AT == 1) load_ff<GU>(X, Bd);
      if (!__all(QOK())) {
        for (unsigned spins = 0;; ++spins) { asm volatile("" ::: "memory"); DELAY(6, BACKOFF); PASS(0 + 5 * layer); QLOADS(); if (__all(QOK()) || spins > SPIN_CAP) break; }
      }
#pragma unroll
      for (int s = 0; s < 4; ++s) { const v2u lo = valH(d[2 * s]), hi = valH(d[2 * s + 1]); qf[s] = v4u{lo.x, lo.y, hi.x, hi.y}; }
    }
    STAMP(2 + 12 * layer);
    f32x4 sc = {0.f, 0.f, 0.f, 0.f};
#pragma unroll
    for (int s = 0; s < 4; ++s) sc = mfma32(Kf[s], qf[s], sc);
    float sv[4];
#pragma unroll
    for (int k = 0; k < 4; k += 2) {
      const unsigned p = pk(sc[k], sc[k + 1]);
      const unsigned r = pk(bflo(p) * 0.08838834764831845f, bfhi(p) * 0.08838834764831845f);
      sv[k] = bflo(r) * 1.4426950408889634f; sv[k + 1] = bfhi(r) * 1.4426950408889634f;
    }
    const float m = xrow_max(fmaxf(fmaxf(sv[0], sv[1]), fmaxf(sv[2], sv[3])));
#pragma unroll
    for (int k = 0; k < 4; ++k) sv[k] = __builtin_amdgcn_exp2f(sv[k] - m);
    const v2u pf = {pk(sv[0], sv[1]), pk(sv[2], sv[3])};
    const float l = xrow_sum((sv[0] + sv[1]) + (sv[2] + sv[3]));
    if (layer == 0) {
      v4u t[4];
#pragma unroll
      for (int n = 0; n < 8; ++n)
#pragma unroll
        for (int jj = 0; jj < 2; ++jj) t[n >> 1][(n & 1) * 2 + jj] = PERM(Vf[2 * jj + 1][n >> 1], Vf[2 * jj][n >> 1], (n & 1) ? 0x07060302u : 0x05040100u);
#pragma unroll
      for (int n = 0; n < 4; ++n) Vf[n] = t[n];
    }
    float o[8];
#pragma unroll
    for (int n = 0; n < 8; ++n) { const f32x4 z = {0.f, 0.f, 0.f, 0.f}; o[n] = mfma16(pf, v2u{Vf[n >> 1][(n & 1) * 2], Vf[n >> 1][(n & 1) * 2 + 1]}, z)[0]; }
    LAS unsigned char* op = L + L_ATT + w * 2048 + kg * 512 + i16 * 32;
    *(LAS f32x4*)op = f32x4{o[0], o[1], o[2], o[3]};
    *(LAS f32x4*)(op + 16) = f32x4{o[4], o[5], o[6], o[7]};
    if (kg == 0 && (i16 & 3) == 0) { *(LAS float*)(L + L_ML + (i16 >> 2) * 64 + w * 4) = m; *(LAS float*)(L + L_ML + (i16 >> 2) * 64 + 32 + w * 4) = l; }
  }
  BARRIER();
  {
    const int h = w >> 1, u = (w & 1) * 16 + (lane >> 2), q4 = lane & 3;
    if (u < 22) {
      const f32x4 m0 = *(LAS f32x4*)(L + L_ML + h * 64), m1 = *(LAS f32x4*)(L + L_ML + h * 64 + 16);
      const f32x4 l0 = *(LAS f32x4*)(L + L_ML + h * 64 + 32), l1 = *(LAS f32x4*)(L + L_ML + h * 64 + 48);
      int d0 = 6 * u + 2 * q4; d0 = d0 > 126 ? 126 : d0;
      f32x2 ov[8];
#pragma unroll
      for (int k = 0; k < 8; ++k) ov[k] = *(LAS f32x2*)(L + L_ATT + k * 2048 + h * 512 + d0 * 4);
      const float M = fmaxf(fmaxf(fmaxf(m0[0], m0[1]), fmaxf(m0[2], m0[3])), fmaxf(fmaxf(m1[0], m1[1]), fmaxf(m1[2], m1[3])));
      float a0 = 0.f, a1 = 0.f, ls = 0.f;
#pragma unroll
      for (int k = 0; k < 8; ++k) {
        const float e = __builtin_amdgcn_exp2f((k < 4 ? m0[k & 3] : m1[k & 3]) - M);
        a0 = fmaf(e, ov[k][0], a0); a1 = fmaf(e, ov[k][1], a1); ls = fmaf(e, k < 4 ? l0[k & 3] : l1[k & 3], ls);
      }
      unsigned dv = pk(a0, a1);
      if (u == 21) dv = q4 == 1 ? __float_as_uint(M) : (q4 == 2 ? __float_as_uint(ls) : dv);
      const unsigned d1 = dppu<0x55>(dv), d2 = dppu<0xAA>(dv);
      if (q4 == 0) st_sc1(rs, WS_O + (((layer * 8 + g) * 4 + h) * 6 + (u >> 2)) * 2048 + c * 64 + (u & 3) * 16, v4u{dv, d1, d2, T});
    }
  }
  STAMP(3 + 12 * layer);
  if (layer == 0) {
#pragma unroll
    for (int n = 0; n < 4; ++n) *(LAS v4u*)(stg + n * 1024 + lane * 16) = Aw[n];
    asm volatile("s_waitcnt lgkmcnt(0)" ::: "memory");
#pragma unroll
    for (int t = 0; t < 6; ++t) {
      const int r0 = 6 * (4 * t + kg) + (i16 >> 2);
      const int ra = r0 > 127 ? 127 : r0, rb = r0 + 4 > 127 ? 127 : r0 + 4;
      const v2u lo = trread(stg + ra * 32 + (i16 & 3) * 8), hi = trread(stg + rb * 32 + (i16 & 3) * 8);
      Aw[t] = v4u{lo.x, lo.y, hi.x, 0u};
    }
    if (kg >= 1) { Aw[5].y = 0u; Aw[5].z = 0u; }
    if (kg >= 2) { Aw[5].x = 0u; }
  }
  {
    const int h = w >> 1, st = w & 1;
    const int obase = WS_O + ((layer * 8 + g) * 4 + h) * 12288;
    const int ooff = obase + (16 * st + i16) * 64 + kg * 16;
    const int o5 = obase + 5 * 2048 + (16 * st + i16) * 64;
    const int ob = obase + 5 * 2048 + (16 * (1 - st) + i16) * 64 + 16;
    DELAY(1 + 8 * layer, D_O);
    v4u d[8];
#pragma unroll
    for (int t = 0; t < 5; ++t) d[t] = ld_sc1(rs, ooff + t * 2048);
    d[5] = ld_sc1(rs, o5 + (kg & 1) * 16); d[6] = ld_sc1(rs, o5 + 16); d[7] = ld_sc1(rs, ob);
    SCHED();
    if (layer == 0 && FF_AT == 2) load_ff<GU>(X, Bd);
#define OOK() ((d[0].w == T) & (d[1].w == T) & (d[2].w == T) & (d[3].w == T) & (d[4].w == T) & (d[5].w == T) & (d[6].w == T) & (d[7].w == T))
    if (!__all(OOK())) {
      for (unsigned spins = 0;; ++spins) {
        asm volatile("" ::: "memory"); DELAY(6, BACKOFF); PASS(1 + 5 * layer);
#pragma unroll
        for (int t = 0; t < 5; ++t) if (d[t].w != T) d[t] = ld_sc1(rs, ooff + t * 2048);
        if (d[5].w != T) d[5] = ld_sc1(rs, o5 + (kg & 1) * 16);
        if (d[6].w != T) d[6] = ld_sc1(rs, o5 + 16);
        if (d[7].w != T) d[7] = ld_sc1(rs, ob);
        if (__all(OOK()) || spins > SPIN_CAP) break;
      }
    }
    STAMP(4 + 12 * layer);
    if (layer == 0 && FF_AT == 3) load_ff<GU>(X, Bd);
    if (kg >= 1) { d[5].y = 0u; d[5].z = 0u; }
    f32x4 acc = {0.f, 0.f, 0.f, 0.f};
#pragma unroll
    for (int t = 0; t < 6; ++t) { d[t].w = 0u; acc = mfma32(Aw[t], d[t], acc); }
    const float mA = __uint_as_float(d[6].y), lA = __uint_as_float(d[6].z), mB = __uint_as_float(d[7].y), lB = __uint_as_float(d[7].z);
    const float M = row_max(fmaxf(mA, mB));
    const float eA = __builtin_amdgcn_exp2f(mA - M), eB = __builtin_amdgcn_exp2f(mB - M);
    const float Ls = row_sum(fmaf(eA, lA, eB * lB));
    const float wgt = eA * __builtin_amdgcn_rcpf(Ls);
    f32x4 y;
#pragma unroll
    for (int r = 0; r < 4; ++r) y[r] = row_sum(acc[r] * wgt);
    if (i16 == 0) *(LAS f32x4*)(red_wo + w * 80 + kg * 16) = y;
  }
  BARRIER();
  if (w == TW) {
    const f32x4 yv = tail_sum(red_wo, lane);
    if (lane < 4) st_sc1(rs, WS_Y + layer * 16384 + g * 2048 + c * 64 + lane * 16, mkF(yv, T));
  }
  STAMP(5 + 12 * layer);
  if (layer == 0) {
    if (GU) {
#pragma unroll
      for (int n = 0; n < 2; ++n) *(LAS v4u*)(stg + n * 1024 + lane * 16) = Bd[n];
      asm volatile("s_waitcnt lgkmcnt(0)" ::: "memory");
      v2u Bg[4];
#pragma unroll
      for (int u = 0; u < 4; ++u) Bg[u] = trread(stg + (16 * u + 4 * kg + (i16 >> 2)) * 32 + (i16 & 3) * 8);
      Bd[0] = v4u{Bg[0].x, Bg[0].y, Bg[1].x, Bg[1].y}; Bd[1] = v4u{Bg[2].x, Bg[2].y, Bg[3].x, Bg[3].y};
      const float cs = colsum64(Bg);
      if (kg == 0) *(LAS float*)(L + L_RED + 2 * RED_SZ + w * 80 + i16 * 4) = cs;
    } else {
#pragma unroll
      for (int n = 0; n < 7; ++n) *(LAS v4u*)(stg + n * 1024 + lane * 16) = Bd[n];
      asm volatile("s_waitcnt lgkmcnt(0)" ::: "memory");
#pragma unroll
      for (int t = 0; t < 7; ++t) {
        const int r0 = 32 * t + 8 * kg + (i16 >> 2);
        const v2u lo = trread(stg + r0 * 32 + (i16 & 3) * 8), hi = trread(stg + (r0 + 4) * 32 + (i16 & 3) * 8);
        Bd[t] = v4u{lo.x, lo.y, hi.x, hi.y};
      }
      asm volatile("s_waitcnt lgkmcnt(0)" ::: "memory");
    }
  }
  if (GU) {
    const int yoff = WS_Y + layer * 16384 + (i16 >> 2) * 2048 + X.U * 16;
    DELAY(2 + 8 * layer, D_Y);
    v4u d0, d1;
    for (unsigned spins = 0;; ++spins) {
      asm volatile("" ::: "memory");
      d0 = ld_sc1(rs, yoff); d1 = ld_sc1(rs, yoff + 8192);
      if (__all(okF(d0, T) & okF(d1, T)) || spins > SPIN_CAP) break;
      DELAY(6, BACKOFF); PASS(2 + 5 * layer);
    }
    STAMP(6 + 12 * layer);
#pragma unroll
    for (int k = 0; k < 4; ++k) {
      float t = __uint_as_float(d0[k] & 0xffffff00u) + __uint_as_float(d1[k] & 0xffffff00u); t += dpp<ROR8>(t); t += dpp<ROR4>(t);
      xres[k] = rbf(xres[k] + t);
    }
    const v2u a = {pk(xres[0], xres[1]), pk(xres[2], xres[3])};
    const v2u Bg[4] = {v2u{Bd[0].x, Bd[0].y}, v2u{Bd[0].z, Bd[0].w}, v2u{Bd[1].x, Bd[1].y}, v2u{Bd[1].z, Bd[1].w}};
    const float part = gemv64(a, Bg);
    const f32x2 st = wave_stats(xres);
    if (kg == 0) *(LAS float*)(red_ff + w * 80 + i16 * 4) = part;
    if (lane == 0) *(LAS f32x2*)(red_ff + w * 80 + 64) = st;
    BARRIER();
    if (w == TW) {
      if (layer == 0) csgu = tail_sum(L + L_RED + 2 * RED_SZ, lane);
      const f32x4 v = tail_sum(red_ff, lane);
      const f32x2 s = tail_stats(red_ff, lane);
      const float mean = s[0] * (1.f / DM), var = fmaxf(fmaf(-mean, mean, s[1] * (1.f / DM)), 0.f);
      const float rstd = __builtin_amdgcn_rsqf(var + 1e-5f);
      float hv[4];
#pragma unroll
      for (int k = 0; k < 4; ++k) {
        const float gv = rstd * fmaf(-mean, csgu[k], v[k]);
        const float uv = dpp<XOR2>(gv);
        hv[k] = uv * __builtin_amdgcn_rcpf(1.f + __builtin_amdgcn_exp2f(-1.4426950408889634f * gv));
      }
      if (lane < 2) st_sc1(rs, WS_H + layer * 8192 + (28 * g + c) * 32 + lane * 16, mkH(hv[0], hv[1], hv[2], hv[3], T));
    }
    STAMP(7 + 12 * layer);
    if (layer == 1) return;
    DELAY(4, D_XG);
  } else {
    if (w == TW) {
      const int yoff = WS_Y + layer * 16384 + ((lane >> 2) & 3) * 2048 + (4 * X.dcol + (lane & 3)) * 16;
      DELAY(2 + 8 * layer, D_Y);
      v4u d0, d1;
      for (unsigned spins = 0;; ++spins) {
        asm volatile("" ::: "memory");
        d0 = ld_sc1(rs, yoff); d1 = ld_sc1(rs, yoff + 8192);
        if (__all(okF(d0, T) & okF(d1, T)) || spins > SPIN_CAP) break;
        DELAY(6, BACKOFF); PASS(2 + 5 * layer);
      }
#pragma unroll
      for (int k = 0; k < 4; ++k) {
        float t = __uint_as_float(d0[k] & 0xffffff00u) + __uint_as_float(d1[k] & 0xffffff00u); t += dpp<ROR8>(t); t += dpp<ROR4>(t);
        xd[k] = rbf(xd[k] + t);
      }
    }
    STAMP(6 + 12 * layer);
    BARRIER();
    const int hoff = WS_H + layer * 8192 + (56 * w + (lane < 56 ? lane : 55)) * 16;
    DELAY(3 + 8 * layer, D_H);
    v4u d;
    for (unsigned spins = 0;; ++spins) {
      asm volatile("" ::: "memory");
      d = ld_sc1(rs, hoff);
      if (__all(okH(d, T)) || spins > SPIN_CAP) break;
      DELAY(6, BACKOFF); PASS(3 + 5 * layer);
    }
    STAMP(8 + 12 * layer);
    *(LAS v2u*)(stg + lane * 8) = valH(d);
    asm volatile("s_waitcnt lgkmcnt(0)" ::: "memory");
    f32x4 a0 = {0.f, 0.f, 0.f, 0.f}, a1 = {0.f, 0.f, 0.f, 0.f};
#pragma unroll
    for (int t = 0; t < 7; ++t) {
      const v4u hf = *(LAS v4u*)(stg + t * 64 + kg * 16);
      if (t & 1) a1 = mfma32(Bd[t], hf, a1); else a0 = mfma32(Bd[t], hf, a0);
    }
    if (i16 == 0) *(LAS f32x4*)(red_ff + w * 80 + kg * 16) = a0 + a1;
    BARRIER();
    if (w == TW) {
      const f32x4 v = tail_sum(red_ff, lane);
#pragma unroll
      for (int k = 0; k < 4; ++k) xd[k] = rbf(xd[k] + v[k]);
      if (layer == 0) { if (lane < 4) st_sc1(rs, WS_X + X.dcol * 64 + lane * 16, mkH(xd[0], xd[1], xd[2], xd[3], T)); }
      else {
        if (lane < 4) *(GAS v2u*)(X.out + 16 * X.dcol + 4 * lane) = v2u{pk(xd[0], xd[1]), pk(xd[2], xd[3])};
        if (X.dcol == 0 && lane == 0) __builtin_amdgcn_raw_buffer_store_b32(X.E + 1u, rs, 0, 0, 16);
      }
    }
    STAMP(9 + 12 * layer);
    if (layer == 1) return;
    DELAY(5, D_XD);
  }
  if (layer == 0) {
    v4u d;
    for (unsigned spins = 0;; ++spins) {
      asm volatile("" ::: "memory");
      d = ld_sc1(rs, WS_X + X.U * 16);
      if (__all(okH(d, T)) || spins > SPIN_CAP) break;
      DELAY(6, BACKOFF); PASS(4);
    }
    STAMP(10);
    const v2u a = valH(d);
    xres = f32x4{bfhi(d.x), bfhi(d.y), bfhi(d.z), bfhi(d.w)};
    const float part = gemv64(a, Bq);
    const f32x2 st = wave_stats(xres);
    LAS unsigned char* red_q = L + L_RED + 5 * RED_SZ;
    if (kg == 0) *(LAS float*)(red_q + w * 80 + i16 * 4) = part;
    if (lane == 0) *(LAS f32x2*)(red_q + w * 80 + 64) = st;
    BARRIER();
    if (w == TW) {
      const f32x4 v = tail_sum(red_q, lane);
      const f32x2 s = tail_stats(red_q, lane);
      const float mean = s[0] * (1.f / DM), var = fmaxf(fmaf(-mean, mean, s[1] * (1.f / DM)), 0.f);
      const float rstd = __builtin_amdgcn_rsqf(var + 1e-5f);
      if (lane < 4) st_sc1(rs, WS_Q + 16384 + g * 2048 + c * 64 + lane * 16,
                           mkH(rstd * fmaf(-mean, csq[0], v[0]), rstd * fmaf(-mean, csq[1], v[1]), rstd * fmaf(-mean, csq[2], v[2]), rstd * fmaf(-mean, csq[3], v[3]), T));
    }
    STAMP(11);
  }
}

__device__ __forceinline__ void run_all(Ctx& X, const u16* x, const u16* kc, const u16* vc, const u16* wq) {
  const int lane = X.lane, w = X.w, kg = X.kg, i16 = X.i16, g = X.g, c = X.c, b = X.b;
  const __amdgpu_buffer_rsrc_t rs = X.rs; LAS unsigned char* L = X.L; LAS unsigned char* stg = X.stg; unsigned char* ws = X.ws;
  (void)b; (void)ws;
  unsigned pfv = 0;
  if (CODE_PF) {
    unsigned long long pc; unsigned len;
    asm volatile("s_getpc_b64 %0\n\tLpf_here:\n\ts_mov_b32 %1, Lpf_end - Lpf_here" : "=s"(pc), "=s"(len));
    unsigned off = (unsigned)(c * 8 + w) * 128u; off = off < len ? off : 0u;
    pfv = *(const GAS unsigned*)(pc + off);
  }
  const unsigned E = __builtin_amdgcn_raw_buffer_load_b32(rs, 0, 0, 16);
  const v4u xf = ldg(x + 8 * lane);
  const v2u xu = *(const GAS v2u*)(x + 4 * X.U);
  v4u wr[2];
#pragma unroll
  for (int n = 0; n < 2; ++n) wr[n] = ldg(wq + (size_t)(64 * w + 32 * n + (lane >> 1)) * 4096 + 512 * g + 16 * c + 8 * (lane & 1));
  SCHED();
  v4u Kf[4], Vf[4], Aw[6], Bd[7];
  const size_t kvbase = ((size_t)g * SK + (SK - WIN) + 128 * c + 16 * w) * DH;
#pragma unroll
  for (int s = 0; s < 4; ++s) Kf[s] = ldg_nt(kc + kvbase + i16 * DH + 32 * s + 8 * kg);
#pragma unroll
  for (int j = 0; j < 4; ++j) Vf[j] = ldg_nt(vc + kvbase + (4 * kg + j) * DH + 8 * i16);
  SCHED();
  if (WO_AT == 0) load_wo(X, Aw);

  const unsigned hsh = (E + 1u) * 0x9E3779B1u;
  const unsigned T = __builtin_amdgcn_readfirstlane((hsh & 0xffffff00u) | (((hsh >> 8) & 0xffu) ^ 0xa5u));
  X.E = E; X.T = T;
  f32x4 xres = {bflo(xu.x), bfhi(xu.x), bflo(xu.y), bfhi(xu.y)};
  v2u Bq[4];
  {
    float xv[8] = {bflo(xf.x), bfhi(xf.x), bflo(xf.y), bfhi(xf.y), bflo(xf.z), bfhi(xf.z), bflo(xf.w), bfhi(xf.w)};
    float s = 0.f;
#pragma unroll
    for (int k = 0; k < 8; ++k) s += xv[k];
    const float mean = xrow_sum(row_sum(s)) * (1.f / DM);
    float v = 0.f;
#pragma unroll
    for (int k = 0; k < 8; ++k) { const float d = xv[k] - mean; v = fmaf(d, d, v); }
    const float var = xrow_sum(row_sum(v)) * (1.f / DM);
    const float mb = rbf(mean), sd = rbf(sqrtf(rbf(rbf(var) + 1e-5f)));
    float xn[4];
#pragma unroll
    for (int k = 0; k < 4; ++k) xn[k] = rbf(xres[k] - mb) / sd;
    const v2u a = {pk(xn[0], xn[1]), pk(xn[2], xn[3])};
#pragma unroll
    for (int n = 0; n < 2; ++n) *(LAS v4u*)(stg + n * 1024 + lane * 16) = wr[n];
    asm volatile("s_waitcnt lgkmcnt(0)" ::: "memory");
#pragma unroll
    for (int u = 0; u < 4; ++u) Bq[u] = trread(stg + (16 * u + 4 * kg + (i16 >> 2)) * 32 + (i16 & 3) * 8);
    const float part = gemv64(a, Bq), cs = colsum64(Bq);
    if (kg == 0) { *(LAS float*)(L + L_RED + 0 * RED_SZ + w * 80 + i16 * 4) = part; *(LAS float*)(L + L_RED + 1 * RED_SZ + w * 80 + i16 * 4) = cs; }
  }
  BARRIER();
  f32x4 csq = {0.f, 0.f, 0.f, 0.f}, csgu = {0.f, 0.f, 0.f, 0.f}, xd = {0.f, 0.f, 0.f, 0.f};
  if (w == TW) {
    const f32x4 qv = tail_sum(L + L_RED + 0 * RED_SZ, lane);
    if (lane < 4) st_sc1(rs, WS_Q + g * 2048 + c * 64 + lane * 16, mkH(qv[0], qv[1], qv[2], qv[3], T));
    csq = tail_sum(L + L_RED + 1 * RED_SZ, lane);
  }
  if (!X.is_gu) {
    const int src = 2 * X.dcol + ((lane & 3) >> 1);
    const unsigned a0 = __shfl(xf.x, src), a1 = __shfl(xf.y, src), a2 = __shfl(xf.z, src), a3 = __shfl(xf.w, src);
    const unsigned t0 = (lane & 1) ? a2 : a0, t1 = (lane & 1) ? a3 : a1;
    xd = f32x4{bflo(t0), bfhi(t0), bflo(t1), bfhi(t1)};
  }
  asm volatile("" :: "v"(pfv));
  STAMP(1);
  if (X.is_gu) {
    run_layer<0, true>(X, Kf, Vf, Bq, Aw, Bd, xres, csq, csgu, xd);
    run_layer<1, true>(X, Kf, Vf, Bq, Aw, Bd, xres, csq, csgu, xd);
  } else {
    run_layer<0, false>(X, Kf, Vf, Bq, Aw, Bd, xres, csq, csgu, xd);
    run_layer<1, false>(X, Kf, Vf, Bq, Aw, Bd, xres, csq, csgu, xd);
  }
}

__global__ void __launch_bounds__(512, 2)
fused_decode(const u16* __restrict__ x, const u16* __restrict__ kc, const u16* __restrict__ vc, const u16* __restrict__ wq,
             const u16* __restrict__ wo, const u16* __restrict__ wg, const u16* __restrict__ wu, const u16* __restrict__ wd,
             u16* __restrict__ out, unsigned char* __restrict__ ws) {
  extern __shared__ __attribute__((aligned(16))) unsigned char smem[];
  Ctx X;
  X.wo = wo; X.wg = wg; X.wu = wu; X.wd = wd; X.out = out; X.ws = ws;
  X.L = (LAS unsigned char*)smem;
  const int tid = threadIdx.x;
  X.lane = tid & 63; X.w = __builtin_amdgcn_readfirstlane(tid >> 6); X.kg = X.lane >> 4; X.i16 = X.lane & 15;
  X.b = blockIdx.x; X.g = X.b & 7; X.c = X.b >> 3; X.is_gu = X.c < 28; X.dcol = X.g * 4 + (X.c - 28);
  X.U = 16 * X.w + 4 * (X.i16 & 3) + X.kg;
  X.rs = __builtin_amdgcn_make_buffer_rsrc((void*)ws, 0, WS_BYTES, 0x00020000);
  X.stg = X.L + L_STG + X.w * STG_W;
  STAMP(0);
  run_all(X, x, kc, vc, wq);
  STAMP_FLUSH();
  if (CODE_PF) asm volatile("Lpf_end:");
}

extern "C" void kernel_launch(void* const* d_in, const int* in_sizes, int n_in, void* d_out, int out_size, void* d_ws, size_t ws_size,
                              hipStream_t stream) {
  (void)in_sizes; (void)n_in; (void)out_size; (void)ws_size;
  (void)hipFuncSetAttribute((const void*)fused_decode, hipFuncAttributeMaxDynamicSharedMemorySize, LDS_BYTES);
  hipLaunchKernelGGL(fused_decode, dim3(256), dim3(512), LDS_BYTES, stream, (const u16*)d_in[0], (const u16*)d_in[1], (const u16*)d_in[2],
                     (const u16*)d_in[3], (const u16*)d_in[4], (const u16*)d_in[6], (const u16*)d_in[7], (const u16*)d_in[8], (u16*)d_out,
                     (unsigned char*)d_ws);
}
```

```cpp
#include <hip/hip_runtime.h>
#include <stdint.h>

typedef unsigned short u16;
typedef unsigned v4u __attribute__((ext_vector_type(4)));
typedef unsigned v2u __attribute__((ext_vector_type(2)));
typedef float f32x4 __attribute__((ext_vector_type(4)));
typedef float f32x2 __attribute__((ext_vector_type(2)));
typedef short s16x4 __attribute__((ext_vector_type(4)));
typedef __bf16 bf16x8 __attribute__((ext_vector_type(8)));
typedef __bf16 bf16x2 __attribute__((ext_vector_type(2)));
#define LAS __attribute__((address_space(3)))
#define GAS __attribute__((address_space(1)))

constexpr int SK = 32768, WIN = 4096, DH = 128, DM = 512, FF = 1792;
constexpr int WS_FO = 4096;
constexpr int WS_FY = WS_FO + 32768;
constexpr int WS_X = WS_FY + 8192;
constexpr int WS_Y = WS_X + 262144;
constexpr int WS_Q0 = WS_Y + 524288;
constexpr int WS_GC = WS_Q0 + 131072;
constexpr int WS_GRP = WS_GC + 65536, GRP_SZ = 143360, O_L = 61440;
constexpr int GO_ML = 0;
constexpr int GO_O = 2048;
constexpr int GO_G = 51200;
constexpr int GO_ST = 59392;
constexpr int GO_ST2 = O_L + 61440 + 8192;
constexpr int GO_Q1 = O_L + 61440;
static_assert(GO_ST2 + 128 <= GRP_SZ && 128 * 480 == O_L, "group block");
constexpr int WS_BYTES = WS_GRP + 8 * GRP_SZ;
constexpr int OOB = 0x7fffff00;
constexpr int WS_STAMP = 8 << 20;
constexpr int L_IMG = 0;
constexpr int L_IGU = 16384;
constexpr int L_IDN = 32768;
constexpr int L_IWO = 40960;
constexpr int L_STG = 57344, STG_W = 8704;
constexpr int L_ATT = L_STG + 4 * STG_W;
constexpr int L_ML = L_ATT + 8192;
constexpr int L_QB = L_ML + 128, QSTR = 320;
constexpr int L_XB = L_QB + 4 * QSTR;
constexpr int XB_SZ = 384;
constexpr int L_HB = L_XB + 3 * XB_SZ;
constexpr int L_FL = L_HB + 1024;
constexpr int L_RED = L_FL + 256, RED_ROW = 144, RED_SZ = 4 * RED_ROW;
constexpr int LDS_BYTES = 112 * 1024;
static_assert(L_RED + 2 * RED_SZ <= LDS_BYTES, "lds");
constexpr int TW = 3;
constexpr unsigned SPIN_CAP = 1u << 17;

constexpr int DL[16] = {  105,   290,   420,   555,   675,   825,   970,   1090,
                          3,   1220,   1,   340,   20,
                          15,   4, 0};

#define SCHED() __builtin_amdgcn_sched_barrier(0)
#define STAMP(i) do {} while (0)
#define PASS(i) do {} while (0)
#define STAMP_FLUSH() do {} while (0)
#define DELAY(i) do { if (DL[i] > 0) __builtin_amdgcn_s_sleep(DL[i]); } while (0)
#define DLV(i) DL[i]
__device__ __forceinline__ unsigned rtc() { return (unsigned)__builtin_amdgcn_s_memrealtime(); }
#define WAIT_AT(ref, i) do { SCHED(); unsigned tg_ = (ref) + (unsigned)DLV(i); const unsigned lo_ = rtc() + (unsigned)DLV(13); if ((int)(lo_ - tg_) > 0) tg_ = lo_; \
    for (int k_ = 0; k_ < 4096 && (int)(rtc() - tg_) < 0; ++k_) {} SCHED(); } while (0)
#define SLIP(i, lat) do { const int late_ = (int)(rtc() - (X.tref + (unsigned)DLV(i) + (unsigned)(lat))); if (late_ > 0) X.tref += (unsigned)late_; } while (0)
#define PW_A 0
#define VPRE 0
#define DN_LATE 1
#define PW_B 1
#define UNLIKELY(x) __builtin_expect(!!(x), 0)
#define LIKELY(x) __builtin_expect(!!(x), 1)
#define PIN(v) asm volatile("" : "+v"(v))

__device__ __forceinline__ float bflo(unsigned u) { return __uint_as_float(u << 16); }
__device__ __forceinline__ float bfhi(unsigned u) { return __uint_as_float(u & 0xffff0000u); }
__device__ __forceinline__ unsigned pk(float lo, float hi) { bf16x2 p = {(__bf16)lo, (__bf16)hi}; return __builtin_bit_cast(unsigned, p); }
__device__ __forceinline__ float rbf(float f) { return bflo((unsigned)__builtin_bit_cast(u16, (__bf16)f)); }
__device__ __forceinline__ float vmax(float a, float b) { return __builtin_elementwise_maximum(a, b); }
template <int CTRL> __device__ __forceinline__ float dpp(float x) {
  return __builtin_bit_cast(float, __builtin_amdgcn_mov_dpp(__builtin_bit_cast(int, x), CTRL, 0xf, 0xf, true));
}
template <int CTRL> __device__ __forceinline__ unsigned dppu(unsigned x) { return (unsigned)__builtin_amdgcn_mov_dpp((int)x, CTRL, 0xf, 0xf, true); }
constexpr int XOR1 = 0xB1, XOR2 = 0x4E, HMIR = 0x141, MIR = 0x140, ROR4 = 0x124, ROR8 = 0x128;
__device__ __forceinline__ float row_sum(float v) { v += dpp<XOR1>(v); v += dpp<XOR2>(v); v += dpp<HMIR>(v); v += dpp<MIR>(v); return v; }
__device__ __forceinline__ float row_max(float v) { v = vmax(v, dpp<XOR1>(v)); v = vmax(v, dpp<XOR2>(v)); v = vmax(v, dpp<HMIR>(v)); v = vmax(v, dpp<MIR>(v)); return v; }
__device__ __forceinline__ float xrow_sum(float x) {
  auto s = __builtin_amdgcn_permlane16_swap(__float_as_uint(x), __float_as_uint(x), false, false);
  x = __uint_as_float(s[0]) + __uint_as_float(s[1]);
  auto t = __builtin_amdgcn_permlane32_swap(__float_as_uint(x), __float_as_uint(x), false, false);
  return __uint_as_float(t[0]) + __uint_as_float(t[1]);
}
__device__ __forceinline__ float xrow_max(float x) {
  auto s = __builtin_amdgcn_permlane16_swap(__float_as_uint(x), __float_as_uint(x), false, false);
  x = vmax(__uint_as_float(s[0]), __uint_as_float(s[1]));
  auto t = __builtin_amdgcn_permlane32_swap(__float_as_uint(x), __float_as_uint(x), false, false);
  return vmax(__uint_as_float(t[0]), __uint_as_float(t[1]));
}
__device__ __forceinline__ float rows_reduce4(f32x4 v) {
  auto a = __builtin_amdgcn_permlane16_swap(__float_as_uint(v[0]), __float_as_uint(v[1]), false, false);
  auto b = __builtin_amdgcn_permlane16_swap(__float_as_uint(v[2]), __float_as_uint(v[3]), false, false);
  const float t01 = __uint_as_float(a[0]) + __uint_as_float(a[1]), t23 = __uint_as_float(b[0]) + __uint_as_float(b[1]);
  auto c = __builtin_amdgcn_permlane32_swap(__float_as_uint(t01), __float_as_uint(t23), false, false);
  return __uint_as_float(c[0]) + __uint_as_float(c[1]);
}
__device__ __forceinline__ f32x4 mfma32(v4u a, v4u b, f32x4 c) {
  return __builtin_amdgcn_mfma_f32_16x16x32_bf16(__builtin_bit_cast(bf16x8, a), __builtin_bit_cast(bf16x8, b), c, 0, 0, 0);
}
__device__ __forceinline__ f32x4 mfma16(v2u a, v2u b, f32x4 c) {
  return __builtin_amdgcn_mfma_f32_16x16x16bf16_1k(__builtin_bit_cast(s16x4, a), __builtin_bit_cast(s16x4, b), c, 0, 0, 0);
}
__device__ __forceinline__ float dot2(unsigned a, unsigned b, float c) {
  return __builtin_amdgcn_fdot2_f32_bf16(__builtin_bit_cast(bf16x2, a), __builtin_bit_cast(bf16x2, b), c, false);
}
__device__ __forceinline__ v2u trread(LAS unsigned char* p) { return __builtin_bit_cast(v2u, __builtin_amdgcn_ds_read_tr16_b64_v4i16((LAS s16x4*)p)); }
__device__ __forceinline__ v4u ldg_nt(const u16* p) { return __builtin_nontemporal_load((const GAS v4u*)p); }
__device__ __forceinline__ v4u ld_sc1(__amdgpu_buffer_rsrc_t rs, int off) { return __builtin_amdgcn_raw_buffer_load_b128(rs, off, 0, 16); }
__device__ __forceinline__ unsigned ld1_sc1(__amdgpu_buffer_rsrc_t rs, int off) { return __builtin_amdgcn_raw_buffer_load_b32(rs, off, 0, 16); }
__device__ __forceinline__ v2u ld2_sc1(__amdgpu_buffer_rsrc_t rs, int off) { return __builtin_amdgcn_raw_buffer_load_b64(rs, off, 0, 16); }
__device__ __forceinline__ void st2_sc1(__amdgpu_buffer_rsrc_t rs, int off, v2u v) { __builtin_amdgcn_raw_buffer_store_b64(v, rs, off, 0, 16); }
__device__ __forceinline__ void st_sc1(__amdgpu_buffer_rsrc_t rs, int off, v4u v) { __builtin_amdgcn_raw_buffer_store_b128(v, rs, off, 0, 16); }
__device__ __forceinline__ void st1_sc1(__amdgpu_buffer_rsrc_t rs, int off, unsigned v) { __builtin_amdgcn_raw_buffer_store_b32(v, rs, off, 0, 16); }
#define PERM(hi, lo, sel) __builtin_amdgcn_perm((hi), (lo), (sel))
__device__ __forceinline__ bool okH(v4u d, unsigned T) { return (PERM(d.y, d.x, 0x05040100u) == T) & (PERM(d.w, d.z, 0x05040100u) == T); }
__device__ __forceinline__ v2u valH(v4u d) { return v2u{PERM(d.y, d.x, 0x07060302u), PERM(d.w, d.z, 0x07060302u)}; }
__device__ __forceinline__ v4u mkH(unsigned p0, unsigned p1, unsigned T) {
  return v4u{PERM(p0, T, 0x05040100u), PERM(p0, T, 0x07060302u), PERM(p1, T, 0x05040100u), PERM(p1, T, 0x07060302u)};
}
__device__ __forceinline__ v4u unF(v4u d, unsigned T) { return v4u{d.x ^ (T & 0xffu), d.y ^ ((T >> 8) & 0xffu), d.z ^ ((T >> 16) & 0xffu), d.w ^ (T >> 24)}; }
__device__ __forceinline__ unsigned or4(v4u d) { return (d.x | d.y) | (d.z | d.w); }
__device__ __forceinline__ v4u mkF(float a, float b, float c, float d, unsigned T) {
  return v4u{PERM(__float_as_uint(a), T, 0x07060500u), PERM(__float_as_uint(b), T, 0x07060501u),
             PERM(__float_as_uint(c), T, 0x07060502u), PERM(__float_as_uint(d), T, 0x07060503u)};
}
#define LGKM0() asm volatile("s_waitcnt lgkmcnt(0)" ::: "memory")
#define BARRIER() do { LGKM0(); __builtin_amdgcn_s_barrier(); asm volatile("" ::: "memory"); } while (0)

__device__ __forceinline__ float gemv128(v2u a0, v2u a1, const v2u (&B)[8]) {
  const f32x4 z = {0.f, 0.f, 0.f, 0.f};
  f32x4 c0 = mfma16(a0, B[0], z), c1 = mfma16(a0, B[1], z), c2 = mfma16(a0, B[2], z), c3 = mfma16(a0, B[3], z);
  c0 = mfma16(a1, B[4], c0); c1 = mfma16(a1, B[5], c1); c2 = mfma16(a1, B[6], c2); c3 = mfma16(a1, B[7], c3);
  return (c0[0] + c1[1]) + (c2[2] + c3[3]);
}
struct Ctx {
  const u16 *vc, *wo, *wg, *wu, *wd; u16* out; unsigned char* ws;
  __amdgpu_buffer_rsrc_t rs; LAS unsigned char* L;
  int lane, w, wv, kg, i16, b, g, c; unsigned E, T, t0, tref;
};

#define LPOST(set) do { asm volatile("" ::: "memory"); *(volatile LAS unsigned*)(L + L_FL + (set) * 16 + w * 4) = T; asm volatile("" ::: "memory"); } while (0)
#define LWAIT4(set) do { for (unsigned spins_ = 0; spins_ < (1u << 24); ++spins_) { const v4u f_ = *(volatile LAS v4u*)(L + L_FL + (set) * 16); \
      if ((f_.x == T) & (f_.y == T) & (f_.z == T) & (f_.w == T)) break; } asm volatile("" ::: "memory"); } while (0)
#define LWAIT1(set, who) do { for (unsigned spins_ = 0; spins_ < (1u << 24); ++spins_) { \
      if (*(volatile LAS unsigned*)(L + L_FL + (set) * 16 + (who) * 4) == T) break; __builtin_amdgcn_s_sleep(1); } asm volatile("" ::: "memory"); } while (0)
#define ONES 0x3f803f80u
#define FL4(set) (*(volatile LAS v4u*)(L + L_FL + (set) * 16))
#define FL4OK(f) (((f).x == T) & ((f).y == T) & ((f).z == T) & ((f).w == T))

__device__ __forceinline__ v4u load_sw(const Ctx& X, int n) {
  const int cb = (X.c & 7) < 7 ? (X.c & 7) : 6;
  if (n < 4) return ldg_nt(X.wo + (size_t)(512 * X.g + 256 * (X.c & 1) + 64 * X.w + 16 * n + (X.lane >> 2)) * DM + 32 * (X.c >> 1) + 8 * (X.lane & 3));
  if (n < 8) return ldg_nt((n >= 6 ? X.wu : X.wg) + (size_t)(128 * (X.c >> 3) + 32 * X.w + 16 * (n & 1) + (X.lane >> 2)) * FF + 224 * X.g + 32 * cb + 8 * (X.lane & 3));
  const int blk = 2 * X.w + (n & 1) < 7 ? 2 * X.w + (n & 1) : 6;
  return ldg_nt(X.wd + (size_t)(224 * X.g + 32 * blk + (X.lane >> 1)) * DM + 16 * X.c + 8 * (X.lane & 1));
}
__device__ __forceinline__ void run_A(Ctx& X, v4u (&Kf)[8], v4u (&Vf)[8], v4u (&Sw)[10]) {
  const int lane = X.lane, w = X.w, kg = X.kg, i16 = X.i16, g = X.g, c = X.c;
  const __amdgpu_buffer_rsrc_t rs = X.rs; LAS unsigned char* L = X.L; LAS unsigned char* stg = L + L_STG + w * STG_W;
  const f32x4 z = {0.f, 0.f, 0.f, 0.f};
  STAMP(12);
  const int u = (lane * 43) >> 7;
  const bool head3 = lane == 3 * u;
  int ooff = head3 ? WS_GRP + g * GRP_SZ + GO_O + w * 12288 + u * 512 + c * 16 : (lane == 1 ? WS_GRP + g * GRP_SZ + GO_ML + w * 512 + c * 16 : OOB);
  const int ostep = (head3 || lane == 1) ? O_L : 0;
  const u16* vb = X.vc + ((size_t)g * SK + (SK - WIN) + 128 * c + 32 * w) * DH;
  {
#pragma unroll
    for (int s = 0; s < 8; ++s) {
      *(LAS v4u*)(stg + (4 * s + kg) * 272 + i16 * 16) = Kf[s];
      SCHED();
      if (s + VPRE < 8) { const int j = s + VPRE; Vf[j] = ldg_nt(vb + (16 * (j >> 2) + 4 * kg + (j & 3)) * DH + 8 * i16); }
      SCHED();
    }
    LGKM0();
#pragma unroll
    for (int s = 0; s < 8; ++s) Kf[s] = *(LAS v4u*)(stg + (16 * (s >> 2) + i16) * 272 + (s & 3) * 64 + kg * 16);
  }
  {
    v4u t[8];
#pragma unroll
    for (int jj = 0; jj < 4; ++jj) {
#pragma unroll
      for (int n = 0; n < 8; ++n) t[n][jj] = PERM(Vf[2 * jj + 1][n >> 1], Vf[2 * jj][n >> 1], (n & 1) ? 0x07060302u : 0x05040100u);
      asm volatile("" : "+v"(t[0][jj]), "+v"(t[7][jj]));
      SCHED();
      Sw[jj] = load_sw(X, jj);
      SCHED();
    }
#pragma unroll
    for (int n = 0; n < 8; ++n) Vf[n] = t[n];
    asm volatile("" :: "v"(Vf[0]), "v"(Vf[1]), "v"(Vf[2]), "v"(Vf[3]), "v"(Vf[4]), "v"(Vf[5]), "v"(Vf[6]), "v"(Vf[7]));
  }
  STAMP(13);
#pragma unroll
  for (int n = 0; n < 4; ++n) { *(LAS v4u*)(L + L_IWO + w * 4096 + n * 1024 + lane * 16) = Sw[n]; SCHED(); Sw[4 + n] = load_sw(X, 4 + n); SCHED(); }
#pragma unroll
  for (int n = 0; n < 4; ++n) {
    *(LAS v4u*)(L + L_IGU + (n >> 1) * 8192 + w * 2048 + (n & 1) * 1024 + lane * 16) = Sw[4 + n]; SCHED();
    if (n & 1) { Sw[8 + (n >> 1)] = load_sw(X, 8 + (n >> 1)); SCHED(); }
  }
  if (DN_LATE == 0) {
#pragma unroll
    for (int n = 0; n < 2; ++n) *(LAS v4u*)(L + L_IDN + (2 * w + n) * 1024 + lane * 16) = Sw[8 + n];
  }
  STAMP(9);
#pragma unroll
  for (int layer = 0; layer < 2; ++layer) {
    BARRIER();
    const unsigned T = __builtin_amdgcn_readfirstlane(*(volatile LAS unsigned*)(L + L_FL));
    {
      v4u qf[4];
#pragma unroll
      for (int s = 0; s < 4; ++s) qf[s] = *(LAS v4u*)(L + L_QB + (i16 >> 2) * QSTR + s * 64 + kg * 16);
      f32x4 sa = z, sb = z;
#pragma unroll
      for (int s = 0; s < 4; ++s) { sa = mfma32(Kf[s], qf[s], sa); sb = mfma32(Kf[4 + s], qf[s], sb); }
      float sv[8];
      if (layer == 0) {
#pragma unroll
        for (int k = 0; k < 8; k += 2) {
          const unsigned p = k < 4 ? pk(sa[k & 3], sa[(k & 3) + 1]) : pk(sb[k & 3], sb[(k & 3) + 1]);
          const unsigned r = pk(bflo(p) * 0.08838834764831845f, bfhi(p) * 0.08838834764831845f);
          sv[k] = bflo(r); sv[k + 1] = bfhi(r);
        }
      } else {
#pragma unroll
        for (int k = 0; k < 4; ++k) { sv[k] = sa[k]; sv[4 + k] = sb[k]; }
      }
      const float CE = layer == 0 ? 1.4426950408889634f : 1.4426950408889634f * 0.08838834764831845f;
      const float m = xrow_max(vmax(vmax(vmax(sv[0], sv[1]), vmax(sv[2], sv[3])), vmax(vmax(sv[4], sv[5]), vmax(sv[6], sv[7])))) * CE;
#pragma unroll
      for (int k = 0; k < 8; ++k) sv[k] = __builtin_amdgcn_exp2f(fmaf(sv[k], CE, -m));
      const v4u pf = {pk(sv[0], sv[1]), pk(sv[2], sv[3]), pk(sv[4], sv[5]), pk(sv[6], sv[7])};
      f32x4 o[8];
#pragma unroll
      for (int n = 0; n < 8; ++n) o[n] = mfma32(pf, Vf[n], z);
      const f32x4 lv = mfma32(pf, v4u{ONES, ONES, ONES, ONES}, z);
      LAS unsigned char* op = L + L_ATT + w * 2048 + kg * 512 + i16 * 32;
      *(LAS f32x4*)op = f32x4{o[0][0], o[1][1], o[2][2], o[3][3]};
      *(LAS f32x4*)(op + 16) = f32x4{o[4][0], o[5][1], o[6][2], o[7][3]};
      if (i16 == 4 * kg) *(LAS f32x2*)(L + L_ML + kg * 32 + w * 8) = f32x2{m, lv[0]};
    }
    LPOST(1 + layer);
    {
      f32x4 ml0, ml1; f32x2 ov[4];
      for (unsigned sp = 0; sp < (1u << 24); ++sp) {
        const v4u f = FL4(1 + layer);
        ml0 = *(volatile LAS f32x4*)(L + L_ML + w * 32); ml1 = *(volatile LAS f32x4*)(L + L_ML + w * 32 + 16);
#pragma unroll
        for (int k = 0; k < 4; ++k) ov[k] = *(volatile LAS f32x2*)(L + L_ATT + k * 2048 + w * 512 + lane * 8);
        if (FL4OK(f)) break;
      }
      const float M = vmax(vmax(ml0[0], ml0[2]), vmax(ml1[0], ml1[2]));
      const float e0 = __builtin_amdgcn_exp2f(ml0[0] - M), e1 = __builtin_amdgcn_exp2f(ml0[2] - M), e2 = __builtin_amdgcn_exp2f(ml1[0] - M), e3 = __builtin_amdgcn_exp2f(ml1[2] - M);
      const f32x2 a = (ov[0] * e0 + ov[1] * e1) + (ov[2] * e2 + ov[3] * e3);
      const float ls = fmaf(e0, ml0[1], e1 * ml0[3]) + fmaf(e2, ml1[1], e3 * ml1[3]);
      unsigned dv = pk(a[0], a[1]);
      unsigned d1 = dppu<0x130>(dv);
      const unsigned d2 = dppu<0x130>(d1);
      dv = head3 ? dv : __float_as_uint(M); d1 = head3 ? d1 : __float_as_uint(ls);
      st_sc1(rs, ooff, v4u{dv, d1, d2, T});
      ooff += ostep; asm volatile("" : "+v"(ooff));
    }
    if (layer == 0) { STAMP(3);
      if (DN_LATE) {
#pragma unroll
        for (int n = 0; n < 2; ++n) *(LAS v4u*)(L + L_IDN + (2 * w + n) * 1024 + lane * 16) = Sw[8 + n];
      }
      LPOST(15);
    } else STAMP(15);
  }
}

#define RETRY(pi) if (LIKELY(__all(ok)) || spins > SPIN_CAP) break; asm volatile("" ::: "memory"); DELAY(8); PASS(pi)
#define LN_STATS(ds) float s1 = __uint_as_float((ds).x), s2 = __uint_as_float((ds).y); \
      s1 += dpp<XOR1>(s1); s2 += dpp<XOR1>(s2); s1 += dpp<XOR2>(s1); s2 += dpp<XOR2>(s2); \
      const float mean = s1 * (1.f / DM), rstd = __builtin_amdgcn_rsqf(fmaxf(fmaf(-mean, mean, s2 * (1.f / DM)), 0.f) + 1e-5f)

template <int layer>
__device__ __forceinline__ void run_B(Ctx& X, const v2u (&Bq)[8], v4u (&Aw)[12], v2u (&Bg)[8], v4u (&Bd)[7], float& xr, f32x4& csq, float (&csg)[2]) {
  const int lane = X.lane, w = X.w, kg = X.kg, i16 = X.i16, g = X.g, c = X.c, kq = c >> 3;
  const unsigned T = X.T; const __amdgpu_buffer_rsrc_t rs = X.rs; LAS unsigned char* L = X.L;
  LAS unsigned char* red = L + L_RED + layer * RED_SZ;
  const f32x4 z = {0.f, 0.f, 0.f, 0.f};
  const int grp = WS_GRP + g * GRP_SZ;
  v2u fy0; v4u c0, c1; unsigned tq;
  const int qun = kg * 8 + (lane & 7), qidx = ((lane >> 3) & 1) * 2048 + (32 * w + qun) * 16;
  const int xun = 8 * w + (lane & 7);
  {
    int qoff = (layer == 0 ? WS_Q0 + g * 8192 : grp + GO_Q1) + qidx, soff = grp + GO_ST2 + (lane & 3) * 16;
    PIN(qoff); PIN(soff);
    if (layer == 0) WAIT_AT(X.t0, 11); else WAIT_AT(X.tref, 4);
    v2u qp;
    for (unsigned spins = 0;; ++spins) {
      v4u ds = v4u{0u, 0u, 0u, T};
      if (layer == 1) ds = ld_sc1(rs, soff);
      v4u d0 = ld_sc1(rs, qoff), d1 = ld_sc1(rs, qoff + 4096);
      SCHED();
      LN_STATS(ds);
      d0 = unF(d0, T); d1 = unF(d1, T);
      float qv[4];
#pragma unroll
      for (int k = 0; k < 4; ++k) {
        float t = __uint_as_float(d0[k]) + __uint_as_float(d1[k]); t += dpp<ROR8>(t);
        qv[k] = layer == 0 ? t : rstd * fmaf(-mean, csq[k], t);
      }
      qp = v2u{pk(qv[0], qv[1]), pk(qv[2], qv[3])};
      *(LAS v2u*)(L + L_QB + w * QSTR + qun * 8) = qp;
      const bool ok = (((or4(d0) | or4(d1)) & 0xffu) == 0u) & (ds.w == T);
      RETRY(layer ? 7 : 0);
    }
    STAMP(2 + 12 * layer);
  }
  BARRIER();
  if (layer == 1) SLIP(4, 75);
  if (layer == 0) {
    tq = rtc();
    if (w == TW && lane == 0) st2_sc1(rs, WS_FO + (g * 32 + c) * 128, v2u{T, tq});
    fy0 = ld2_sc1(rs, w == TW ? WS_FO + (g * 32 + (lane & 31)) * 128 : OOB);
    c0 = ld_sc1(rs, WS_Q0 + 65536 + g * 8192 + qidx); c1 = ld_sc1(rs, WS_Q0 + 65536 + g * 8192 + qidx + 4096);
    SCHED();
  }
#define WO_PREP() do { const int hh = w >> 1; \
_Pragma("unroll") \
    for (int t = 0; t < 6; ++t) { \
      const int un = 4 * t + kg, r0 = 6 * un + (i16 >> 2); \
      const int ra = r0 > 127 ? 127 : r0, rb = r0 + 4 > 127 ? 127 : r0 + 4; \
_Pragma("unroll") \
      for (int ct = 0; ct < 2; ++ct) { \
        const v2u lo = trread(L + L_IWO + (hh * 128 + ra) * 64 + ct * 32 + (i16 & 3) * 8), hi = trread(L + L_IWO + (hh * 128 + rb) * 64 + ct * 32 + (i16 & 3) * 8); \
        Aw[t * 2 + ct] = v4u{(t == 5 && un > 21) ? 0u : lo.x, (t == 5 && un > 20) ? 0u : lo.y, (t == 5 && un > 20) ? 0u : hi.x, 0u}; \
      } \
    } \
  } while (0)
  if (layer == 0) {
    WO_PREP();
#pragma unroll
    for (int u = 0; u < 8; ++u) Bg[u] = trread(L + L_IGU + (w >> 1) * 8192 + (64 * (u >> 2) + 16 * kg + 4 * (u & 3) + (i16 >> 2)) * 64 + (w & 1) * 32 + (i16 & 3) * 8);
    const v2u one = {ONES, ONES};
    const float cs = gemv128(one, one, Bg);
    const float p1 = dpp<0x55>(cs), p2 = dpp<0xAA>(cs), p3 = dpp<0xFF>(cs);
    st_sc1(rs, ((lane & 0x33) == 0 && (c & 7) < 7) ? WS_GC + g * 8192 + kq * 2048 + (w >> 1) * 1024 + (8 * (c & 7) + 4 * (w & 1) + (i16 >> 2)) * 16 : OOB, mkF(cs, p1, p2, p3, T));

  }
  {
    const int hh = w >> 1, st = w & 1, h = 2 * (c & 1) + hh;
    int obase = grp + GO_O + layer * O_L + h * 12288 + (16 * st + i16) * 16 + kg * 512;
    int o5 = obase + 5 * 2048 - (kg & 2) * 512;
    const int rot = (4 * kg + i16) & 15;
    int mlA = grp + GO_ML + layer * O_L + h * 512 + (16 * st + rot) * 16, mlB = mlA ^ 256;
    const int slot = ((lane >> 4) & 1) * 4 + (lane & 3), rep = ((lane >> 2) & 3) + 4 * (lane >> 5);
    int yst = WS_Y + layer * 262144 + rep * 32768 + (2 * g + (c & 1)) * 2048 + (c >> 1) * 128 + slot * 16;
    PIN(obase); PIN(o5); PIN(mlA); PIN(mlB); PIN(yst);
    if (layer == 0) {
      unsigned tg;
      if (w == TW) {
        for (unsigned spins = 0;; ++spins) {
          if ((lane & 31) == c) fy0 = v2u{T, tq};
          if (__all(fy0.x == T) || spins > SPIN_CAP) break;
          asm volatile("" ::: "memory"); DELAY(10); PASS(1);
          fy0 = ld2_sc1(rs, WS_FO + (g * 32 + (lane & 31)) * 128);
        }
        int dt = (int)(fy0.y - X.t0); dt = max(dt, (int)dppu<XOR1>((unsigned)dt)); dt = max(dt, (int)dppu<XOR2>((unsigned)dt)); dt = max(dt, (int)dppu<HMIR>((unsigned)dt));
        dt = max(dt, (int)dppu<MIR>((unsigned)dt)); dt = max(__builtin_amdgcn_readlane(dt, 0), __builtin_amdgcn_readlane(dt, 16)); tg = X.t0 + (unsigned)dt;
        *(volatile LAS unsigned*)(L + L_FL + 3 * 16) = tg; LPOST(3);
      } else { LWAIT1(3, TW); tg = __builtin_amdgcn_readfirstlane(*(volatile LAS unsigned*)(L + L_FL + 3 * 16)); }
      if (c == 0 && w == TW && lane < 8) st2_sc1(rs, WS_FY + (lane * 8 + g) * 128, v2u{T, tg});
      for (unsigned spins = 0;; ++spins) {
        c0 = unF(c0, T); c1 = unF(c1, T);
        if (__all(((or4(c0) | or4(c1)) & 0xffu) == 0u) || spins > SPIN_CAP) break;
        asm volatile("" ::: "memory"); DELAY(8); c0 = ld_sc1(rs, WS_Q0 + 65536 + g * 8192 + qidx); c1 = ld_sc1(rs, WS_Q0 + 65536 + g * 8192 + qidx + 4096);
      }
#pragma unroll
      for (int k = 0; k < 4; ++k) { float t = __uint_as_float(c0[k]) + __uint_as_float(c1[k]); t += dpp<ROR8>(t); csq[k] = t; }
      { unsigned t_ = tg + (unsigned)DLV(0); for (int k_ = 0; k_ < 4096 && (int)(rtc() - t_) < 0; ++k_) {} }
      X.tref = tg;
    } else WAIT_AT(X.tref, 5);
    if (layer == 0) fy0 = ld2_sc1(rs, WS_FY + (g * 8 + (lane & 7)) * 128);
    for (unsigned spins = 0;; ++spins) {
      v4u d[8];
      d[6] = ld_sc1(rs, mlA); d[7] = ld_sc1(rs, mlB);
#pragma unroll
      for (int t = 0; t < 5; ++t) d[t] = ld_sc1(rs, obase + t * 2048);
      d[5] = ld_sc1(rs, o5);
      SCHED();
      const float mA = __uint_as_float(d[6].x), lA = __uint_as_float(d[6].y), mB = __uint_as_float(d[7].x), lB = __uint_as_float(d[7].y);
      const float M = row_max(vmax(mA, mB));
      const float eA = __builtin_amdgcn_exp2f(mA - M), eB = __builtin_amdgcn_exp2f(mB - M);
      const float Ls = row_sum(fmaf(eA, lA, eB * lB));
      const float wgt = eA * __builtin_amdgcn_rcpf(Ls);
      const float w0 = dpp<0x150>(wgt), w1 = dpp<0x151>(wgt), w2 = dpp<0x152>(wgt), w3 = dpp<0x153>(wgt);
      f32x4 a0 = z, a1 = z;
#pragma unroll
      for (int t = 0; t < 6; ++t) { a0 = mfma32(d[t], Aw[2 * t], a0); a1 = mfma32(d[t], Aw[2 * t + 1], a1); }
      const float y0 = fmaf(a0[0], w0, a0[1] * w1) + fmaf(a0[2], w2, a0[3] * w3), y1 = fmaf(a1[0], w0, a1[1] * w1) + fmaf(a1[2], w2, a1[3] * w3);
      auto sw = __builtin_amdgcn_permlane16_swap(__float_as_uint(y0), __float_as_uint(y1), false, false);
      const float yy = __uint_as_float(sw[0]) + __uint_as_float(sw[1]);
      auto sx = __builtin_amdgcn_permlane32_swap(__float_as_uint(yy), __float_as_uint(yy), false, false);
      *(LAS float*)(red + w * RED_ROW + (lane & 31) * 4) = __uint_as_float(sx[0]) + __uint_as_float(sx[1]);
      const bool ok = (d[0].w == T) & (d[1].w == T) & (d[2].w == T) & (d[3].w == T) & (d[4].w == T) & (d[5].w == T) & (d[6].w == T) & (d[7].w == T);
      RETRY(layer ? 8 : 2);
    }
    if (layer == 0) asm volatile("s_waitcnt vmcnt(0)" ::: "memory");
    LPOST(4 + 5 * layer);
    STAMP(4 + 12 * layer);
    if (w == TW) {
      f32x4 v;
      for (unsigned sp = 0; sp < (1u << 24); ++sp) {
        const v4u f = FL4(4 + 5 * layer);
        v = *(volatile LAS f32x4*)(red + ((lane >> 2) & 3) * RED_ROW + slot * 16);
        if (FL4OK(f)) break;
      }
#pragma unroll
      for (int k = 0; k < 4; ++k) { float t = v[k]; t += dpp<ROR8>(t); t += dpp<ROR4>(t); v[k] = t; }
      st_sc1(rs, yst, mkF(v[0], v[1], v[2], v[3], T));
    }
  }
  STAMP(5 + 12 * layer);
  if (layer == 1) SLIP(5, 125);
#define X_SHARE(xb, setx, sets) do { LPOST(setx); \
    float s1 = xr, s2 = xr * xr; \
    s1 += dpp<XOR1>(s1); s2 += dpp<XOR1>(s2); s1 += dpp<XOR2>(s1); s2 += dpp<XOR2>(s2); s1 += dpp<HMIR>(s1); s2 += dpp<HMIR>(s2); \
    *(LAS f32x2*)((xb) + 256 + (4 * w + kg) * 8) = f32x2{s1, s2}; LPOST(sets); } while (0)
#define X_FETCH(xb, setx) v2u a0, a1; for (unsigned sp = 0; sp < (1u << 24); ++sp) { const v4u f = FL4(setx); \
      a0 = *(volatile LAS v2u*)((xb) + (4 * kg + (i16 & 3)) * 8); a1 = *(volatile LAS v2u*)((xb) + 128 + (4 * kg + (i16 & 3)) * 8); if (FL4OK(f)) break; }
#define STATS_PUBLISH(xb, sets, idx) do { f32x2 sp2; for (unsigned sp = 0; sp < (1u << 24); ++sp) { const v4u f = FL4(sets); sp2 = *(volatile LAS f32x2*)((xb) + 256 + i16 * 8); if (FL4OK(f)) break; } \
    const float S1 = row_sum(sp2[0]), S2 = row_sum(sp2[1]); \
    st_sc1(rs, lane == 0 ? grp + (idx) + kq * 16 : OOB, v4u{__float_as_uint(S1), __float_as_uint(S2), 0u, T}); } while (0)
  {
    const int go = kq * 2048 + (w >> 1) * 1024 + (8 * (c & 7) + 4 * (w & 1) + (i16 >> 2)) * 16;
    const bool gst = (lane & 0x33) == 0 && (c & 7) < 7;
    int goff = gst ? grp + GO_G + layer * O_L + go : OOB;
    int yoff = WS_Y + layer * 262144 + g * 32768 + (lane >> 3) * 2048 + (32 * kq + xun) * 16;
    PIN(goff); PIN(yoff);
    if (layer == 0) {
      {
        for (unsigned spins = 0;; ++spins) {
          if ((lane & 7) == g) fy0 = v2u{T, X.tref};
          if (__all(fy0.x == T) || spins > SPIN_CAP) break;
          asm volatile("" ::: "memory"); DELAY(14); PASS(3);
          fy0 = ld2_sc1(rs, WS_FY + (g * 8 + (lane & 7)) * 128);
        }
        int dt = (int)(fy0.y - X.t0); dt = max(dt, (int)dppu<XOR1>((unsigned)dt)); dt = max(dt, (int)dppu<XOR2>((unsigned)dt)); dt = max(dt, (int)dppu<HMIR>((unsigned)dt));
        X.tref = X.t0 + (unsigned)__builtin_amdgcn_readfirstlane(dt);
      }
      { unsigned t_ = X.tref + (unsigned)DLV(1); for (int k_ = 0; k_ < 4096 && (int)(rtc() - t_) < 0; ++k_) {} }
    } else WAIT_AT(X.tref, 6);
    LAS unsigned char* xb = L + L_XB + 2 * layer * XB_SZ;
    float xn;
    for (unsigned spins = 0;; ++spins) {
      v4u d0 = ld_sc1(rs, yoff), d1 = ld_sc1(rs, yoff + 16384);
      SCHED();
      d0 = unF(d0, T); d1 = unF(d1, T);
      float t = rows_reduce4(__builtin_bit_cast(f32x4, d0) + __builtin_bit_cast(f32x4, d1));
      t += dpp<ROR8>(t);
      const unsigned p = pk(xr + t, 0.f);
      *(LAS u16*)(xb + (4 * xun + kg) * 2) = (u16)p;
      xn = bflo(p);
      const bool ok = ((or4(d0) | or4(d1)) & 0xffu) == 0u;
      RETRY(layer ? 9 : 4);
    }
    xr = xn;
    STAMP(6 + 12 * layer);
    X_SHARE(xb, 6 + 4 * layer, 12 + layer);
    if ((c & 7) < 7) {
      X_FETCH(xb, 6 + 4 * layer);
      const float part = gemv128(a0, a1, Bg);
      const float p1 = dpp<0x55>(part), p2 = dpp<0xAA>(part), p3 = dpp<0xFF>(part);
      st_sc1(rs, goff, mkF(part, p1, p2, p3, T));
    } else if (w == 0) STATS_PUBLISH(xb, 12 + layer, GO_ST + layer * O_L);
  }
  if (layer == 1) STAMP(19);
  SLIP(layer == 0 ? 1 : 6, 100);
  if (layer == 0) {
    const v4u zz = {0u, 0u, 0u, 0u};
    const int slot = ((lane >> 4) & 1) * 4 + (lane & 3), rep = ((lane >> 2) & 3) + 4 * (lane >> 5);
    if (w == TW) {
      st_sc1(rs, WS_Y + 262144 + rep * 32768 + (2 * g + (c & 1)) * 2048 + (c >> 1) * 128 + slot * 16, zz);
      st_sc1(rs, i16 < 8 ? WS_X + 131072 + i16 * 16384 + g * 2048 + c * 64 + kg * 16 : OOB, zz);
    }
    st_sc1(rs, lane < 30 ? grp + O_L + (c * 4 + w) * 480 + lane * 16
                         : ((lane & 0x33) == 0x20 ? grp + GO_Q1 + kq * 2048 + ((c & 7) * 64 + w * 16 + i16) * 4 : ((lane == 63 && (c & 7) == 0 && w == 0) ? grp + GO_ST2 + kq * 16 : OOB)), zz);
  }
  {
    const int hu = 14 * w + (i16 < 14 ? i16 : 13);
    const int gi = kg * 2048 + hu * 16;
    int gld = grp + GO_G + layer * O_L + gi, soff = grp + GO_ST + layer * O_L + (lane & 3) * 16;
    int xst = i16 < 8 ? WS_X + layer * 131072 + i16 * 16384 + g * 2048 + c * 64 + kg * 16 : OOB;
    PIN(gld); PIN(soff); PIN(xst);
    if (layer == 0 && w == TW) {
      LWAIT4(15);
#pragma unroll
      for (int t = 0; t < 7; ++t) {
        const int r0 = 32 * t + 8 * kg + (i16 >> 2);
        const v2u lo = trread(L + L_IDN + r0 * 32 + (i16 & 3) * 8), hi = trread(L + L_IDN + (r0 + 4) * 32 + (i16 & 3) * 8);
        Bd[t] = v4u{lo.x, lo.y, hi.x, hi.y};
      }
    }
    WAIT_AT(X.tref, layer == 0 ? 2 : 7);
    LAS unsigned char* hb = L + L_HB + layer * 512;
    for (unsigned spins = 0;; ++spins) {
      const v4u ds = ld_sc1(rs, soff);
      if (layer == 0) { c0 = ld_sc1(rs, WS_GC + g * 8192 + gi); c1 = ld_sc1(rs, WS_GC + g * 8192 + gi + 1024); }
      v4u d0 = ld_sc1(rs, gld), d1 = ld_sc1(rs, gld + 1024);
      SCHED();
      LN_STATS(ds);
      unsigned cbad = 0u;
      if (layer == 0) {
        c0 = unF(c0, T); c1 = unF(c1, T); cbad = or4(c0) | or4(c1);
        csg[0] = rows_reduce4(__builtin_bit_cast(f32x4, c0)); csg[1] = rows_reduce4(__builtin_bit_cast(f32x4, c1));
      }
      d0 = unF(d0, T); d1 = unF(d1, T);
      const float gv = rstd * fmaf(-mean, csg[0], rows_reduce4(__builtin_bit_cast(f32x4, d0)));
      const float uv = rstd * fmaf(-mean, csg[1], rows_reduce4(__builtin_bit_cast(f32x4, d1)));
      const float hv = uv * __builtin_amdgcn_rcpf(1.f + __builtin_amdgcn_exp2f(-1.4426950408889634f * gv));
      *(LAS u16*)(hb + (4 * hu + kg) * 2) = (u16)pk(hv, 0.f);
      const bool ok = (((or4(d0) | or4(d1) | cbad) & 0xffu) == 0u) & (ds.w == T);
      RETRY(5);
    }
    if (layer == 0) asm volatile("s_waitcnt vmcnt(0)" ::: "memory");
    LPOST(7 + 4 * layer);
    STAMP(8 + 12 * layer);
    if (w == TW) {
      v4u hf[7];
      for (unsigned sp = 0; sp < (1u << 24); ++sp) {
        const v4u f = FL4(7 + 4 * layer);
#pragma unroll
        for (int t = 0; t < 7; ++t) hf[t] = *(volatile LAS v4u*)(hb + t * 64 + kg * 16);
        if (FL4OK(f)) break;
      }
      f32x4 a0 = z, a1 = z;
#pragma unroll
      for (int t = 0; t < 7; ++t) { if (t & 1) a1 = mfma32(Bd[t], hf[t], a1); else a0 = mfma32(Bd[t], hf[t], a0); }
      a0 += a1;
      st_sc1(rs, xst, mkF(a0[0], a0[1], a0[2], a0[3], T));
    }
    if (layer == 0) STAMP(10);
    SLIP(layer == 0 ? 2 : 7, 108);
  }
  if (layer == 0 || (g == 0 && (c & 7) == 0)) {
    int xoff = WS_X + layer * 131072 + g * 16384 + (lane >> 3) * 2048 + (32 * kq + xun) * 16;
    int qst = (lane & 0x33) == 0 ? grp + GO_Q1 + kq * 2048 + ((c & 7) * 64 + w * 16 + i16) * 4 : OOB;
    PIN(xoff); PIN(qst);
    WAIT_AT(X.tref, layer == 0 ? 3 : 9);
    LAS unsigned char* xb = L + L_XB + XB_SZ;
    unsigned p;
    for (unsigned spins = 0;; ++spins) {
      const v4u d0 = unF(ld_sc1(rs, xoff), T);
      float t = rows_reduce4(__builtin_bit_cast(f32x4, d0));
      t += dpp<ROR8>(t);
      p = pk(xr + t, 0.f);
      if (layer == 0) *(LAS u16*)(xb + (4 * xun + kg) * 2) = (u16)p;
      const bool ok = (or4(d0) & 0xffu) == 0u;
      RETRY(6);
    }
    if (layer == 0) {
      xr = bflo(p);
      X_SHARE(xb, 8, 14);
      X_FETCH(xb, 8);
      const float part = gemv128(a0, a1, Bq);
      const float p1 = dpp<0x55>(part), p2 = dpp<0xAA>(part), p3 = dpp<0xFF>(part);
      st_sc1(rs, qst, mkF(part, p1, p2, p3, T));
      if ((c & 7) == 0 && w == 0) STATS_PUBLISH(xb, 14, GO_ST2);
      STAMP(11);
      SLIP(3, 100);
    } else {
      if ((lane & 8) == 0) *(GAS u16*)(X.out + 128 * kq + 4 * xun + kg) = (u16)p;
      if (c == 0 && w == 0 && lane == 0) st1_sc1(rs, 0, X.E + 1u);
      STAMP(21);
    }
  }
}

__global__ void __launch_bounds__(512, 2)
fused_decode(const u16* __restrict__ x, const u16* __restrict__ kc, const u16* __restrict__ vc, const u16* __restrict__ wq,
             const u16* __restrict__ wo, const u16* __restrict__ wg, const u16* __restrict__ wu, const u16* __restrict__ wd,
             u16* __restrict__ out, unsigned char* __restrict__ ws) {
  extern __shared__ __attribute__((aligned(16))) unsigned char smem[];
  Ctx X;
  X.vc = vc; X.wo = wo; X.wg = wg; X.wu = wu; X.wd = wd; X.out = out; X.ws = ws;
  X.L = (LAS unsigned char*)smem;
  const int tid = threadIdx.x;
  X.lane = tid & 63; X.wv = __builtin_amdgcn_readfirstlane(tid >> 6); X.w = X.wv & 3; X.kg = X.lane >> 4; X.i16 = X.lane & 15;
  X.b = blockIdx.x; X.g = X.b & 7; X.c = X.b >> 3;
  X.rs = __builtin_amdgcn_make_buffer_rsrc((void*)ws, 0, WS_BYTES, 0x00020000);
  STAMP(0);
  X.t0 = rtc();
  const int lane = X.lane, w = X.w, kg = X.kg, i16 = X.i16, g = X.g, c = X.c, kq = c >> 3;
  const __amdgpu_buffer_rsrc_t rs = X.rs; LAS unsigned char* L = X.L;
  unsigned pfv;
  {
    unsigned long long pc; unsigned len;
    asm volatile("s_getpc_b64 %0\n\tLpf_here:\n\ts_mov_b32 %1, Lpf_end - Lpf_here" : "=s"(pc), "=s"(len));
    unsigned off = (unsigned)(c * 8 + X.wv) * 128u; off = off < len ? off : 0u;
    pfv = *(const GAS unsigned*)(pc + off);
  }
  SCHED();
  asm volatile("" :: "s"(x), "s"(kc), "s"(vc), "s"(wq), "s"(wo), "s"(wg), "s"(wu), "s"(wd), "s"(out), "s"(ws));
  v4u Kf[8], Vf[8], Sw[10], wr[4], xf; v2u xa, xb; unsigned xu, E;
  if (X.wv >= 4) {
    DELAY(12);
    const size_t kvbase = ((size_t)g * SK + (SK - WIN) + 128 * c + 32 * w) * DH;
#pragma unroll
    for (int s = 0; s < 8; ++s) Kf[s] = ldg_nt(kc + kvbase + s * 512 + lane * 8);
#pragma unroll
    for (int j = 0; j < VPRE; ++j) Vf[j] = ldg_nt(vc + kvbase + (16 * (j >> 2) + 4 * kg + (j & 3)) * DH + 8 * i16);
  } else {
#pragma unroll
    for (int n = 0; n < 4; ++n) wr[n] = ldg_nt(wq + (size_t)(128 * kq + 32 * w + 8 * n + (lane >> 3)) * 4096 + 512 * g + 64 * (c & 7) + 8 * (lane & 7));
    xf = *(const GAS v4u*)(x + 8 * lane);
    xa = *(const GAS v2u*)(x + 4 * (32 * kq + 4 * kg + (i16 & 3))); xb = *(const GAS v2u*)(x + 4 * (32 * kq + 16 + 4 * kg + (i16 & 3)));
    xu = *(const GAS u16*)(x + 128 * kq + 4 * (8 * w + (lane & 7)) + kg);
    E = ld1_sc1(rs, 0);
    SCHED();
    {
      const v4u zz = {0u, 0u, 0u, 0u};
      const int slot = ((lane >> 4) & 1) * 4 + (lane & 3), rep = ((lane >> 2) & 3) + 4 * (lane >> 5), grp = WS_GRP + g * GRP_SZ;
      if (w == TW) {
        st_sc1(rs, WS_Y + rep * 32768 + (2 * g + (c & 1)) * 2048 + (c >> 1) * 128 + slot * 16, zz);
        st_sc1(rs, i16 < 8 ? WS_X + i16 * 16384 + g * 2048 + c * 64 + kg * 16 : OOB, zz);
      }
      st_sc1(rs, (c & 7) < 7 ? ((lane & 0x33) == 0 ? grp + GO_G + kq * 2048 + (w >> 1) * 1024 + (8 * (c & 7) + 4 * (w & 1) + (i16 >> 2)) * 16 : OOB)
                             : ((w == 0 && lane == 0) ? grp + GO_ST + kq * 16 : OOB), zz);
    }
  }
  SCHED();
  int wv2 = X.wv; asm volatile("" : "+s"(wv2));
  if (wv2 >= 4) run_A(X, Kf, Vf, Sw);
  int wv3 = X.wv; asm volatile("" : "+s"(wv3));
  if (wv3 < 4) {
#pragma unroll
    for (int n = 0; n < 4; ++n) *(LAS v4u*)(L + L_IMG + w * 4096 + n * 1024 + lane * 16) = wr[n];
    const unsigned hsh = (E + 1u) * 0x9E3779B1u;
    const unsigned T = __builtin_amdgcn_readfirstlane(((hsh & 0xbfffbf00u) | (((hsh >> 8) & 0xbfu) ^ 0xa5u)));
    X.E = E; X.T = T;
    LPOST(0);
    float xr = bflo(xu);
    v2u Bq[8], Bg[8]; v4u Aw[12], Bd[7];
    {
      float xv[8] = {bflo(xf.x), bfhi(xf.x), bflo(xf.y), bfhi(xf.y), bflo(xf.z), bfhi(xf.z), bflo(xf.w), bfhi(xf.w)};
      float s = 0.f;
#pragma unroll
      for (int k = 0; k < 8; ++k) s += xv[k];
      const float mean = xrow_sum(row_sum(s)) * (1.f / DM);
      float v = 0.f;
#pragma unroll
      for (int k = 0; k < 8; ++k) { const float d = xv[k] - mean; v = fmaf(d, d, v); }
      const float var = xrow_sum(row_sum(v)) * (1.f / DM);
      const float mb = rbf(mean), isd = 1.0f / rbf(sqrtf(rbf(rbf(var) + 1e-5f)));
      const float xi[8] = {bflo(xa.x), bfhi(xa.x), bflo(xa.y), bfhi(xa.y), bflo(xb.x), bfhi(xb.x), bflo(xb.y), bfhi(xb.y)};
      float xn[8];
#pragma unroll
      for (int k = 0; k < 8; ++k) xn[k] = rbf(xi[k] - mb) * isd;
      const v2u a0 = {pk(xn[0], xn[1]), pk(xn[2], xn[3])}, a1 = {pk(xn[4], xn[5]), pk(xn[6], xn[7])};
      LWAIT4(0);
#pragma unroll
      for (int u = 0; u < 8; ++u) Bq[u] = trread(L + L_IMG + (64 * (u >> 2) + 16 * kg + 4 * (u & 3) + (i16 >> 2)) * 128 + w * 32 + (i16 & 3) * 8);
      const int qo = (lane & 0x33) == 0 ? WS_Q0 + g * 8192 + kq * 2048 + ((c & 7) * 64 + w * 16 + i16) * 4 : OOB - 65536;
      {
        const float part = gemv128(a0, a1, Bq);
        const float p1 = dpp<0x55>(part), p2 = dpp<0xAA>(part), p3 = dpp<0xFF>(part);
        st_sc1(rs, qo, mkF(part, p1, p2, p3, T));
      }
      STAMP(1);
      {
        const v2u one = {ONES, ONES};
        const float part = gemv128(one, one, Bq);
        const float p1 = dpp<0x55>(part), p2 = dpp<0xAA>(part), p3 = dpp<0xFF>(part);
        st_sc1(rs, qo + 65536, mkF(part, p1, p2, p3, T));
      }
    }
    f32x4 csq = {0.f, 0.f, 0.f, 0.f}; float csg[2] = {0.f, 0.f};
    run_B<0>(X, Bq, Aw, Bg, Bd, xr, csq, csg);
    run_B<1>(X, Bq, Aw, Bg, Bd, xr, csq, csg);
  }
  asm volatile("" :: "v"(pfv));
  STAMP_FLUSH();
  asm volatile("Lpf_end:");
}

extern "C" void kernel_launch(void* const* d_in, const int* in_sizes, int n_in, void* d_out, int out_size, void* d_ws, size_t ws_size,
                              hipStream_t stream) {
  (void)in_sizes; (void)n_in; (void)out_size; (void)ws_size;
  (void)hipFuncSetAttribute((const void*)fused_decode, hipFuncAttributeMaxDynamicSharedMemorySize, LDS_BYTES);
  hipLaunchKernelGGL(fused_decode, dim3(256), dim3(512), LDS_BYTES, stream, (const u16*)d_in[0], (const u16*)d_in[1], (const u16*)d_in[2],
                     (const u16*)d_in[3], (const u16*)d_in[4], (const u16*)d_in[6], (const u16*)d_in[7], (const u16*)d_in[8], (u16*)d_out,
                     (unsigned char*)d_ws);
}
```

```cpp
#include <hip/hip_runtime.h>
#include <stdint.h>

typedef unsigned short u16;
typedef unsigned v4u __attribute__((ext_vector_type(4)));
typedef unsigned v2u __attribute__((ext_vector_type(2)));
typedef float f32x4 __attribute__((ext_vector_type(4)));
typedef float f32x2 __attribute__((ext_vector_type(2)));
typedef short s16x4 __attribute__((ext_vector_type(4)));
typedef __bf16 bf16x8 __attribute__((ext_vector_type(8)));
typedef __bf16 bf16x2 __attribute__((ext_vector_type(2)));
#define LAS __attribute__((address_space(3)))
#define GAS __attribute__((address_space(1)))

constexpr int SK = 32768, WIN = 4096, DH = 128, DM = 512, FF = 1792;
constexpr int WS_FO = 4096;
constexpr int WS_FY = WS_FO + 32768;
constexpr int WS_X = WS_FY + 8192;
constexpr int WS_Y = WS_X + 262144;
constexpr int WS_Q0 = WS_Y + 524288;
constexpr int WS_GC = WS_Q0 + 131072;
constexpr int WS_GRP = WS_GC + 65536, GRP_SZ = 143360, O_L = 61440;
constexpr int GO_ML = 0;
constexpr int GO_O = 2048;
constexpr int GO_G = 51200;
constexpr int GO_ST = 59392;
constexpr int GO_ST2 = O_L + 61440 + 8192;
constexpr int GO_Q1 = O_L + 61440;
static_assert(GO_ST2 + 128 <= GRP_SZ && 128 * 480 == O_L, "group block");
constexpr int WS_BYTES = WS_GRP + 8 * GRP_SZ;
constexpr int OOB = 0x7fffff00;
constexpr int WS_STAMP = 8 << 20;
constexpr int L_IMG = 0;
constexpr int L_IGU = 16384;
constexpr int L_IDN = 32768;
constexpr int L_IWO = 40960;
constexpr int L_STG = 57344, STG_W = 8704;
constexpr int L_ATT = L_STG + 4 * STG_W;
constexpr int L_ML = L_ATT + 8192;
constexpr int L_QB = L_ML + 128, QSTR = 320;
constexpr int L_XB = L_QB + 4 * QSTR;
constexpr int XB_SZ = 384;
constexpr int L_HB = L_XB + 3 * XB_SZ;
constexpr int L_FL = L_HB + 1024;
constexpr int L_RED = L_FL + 256, RED_ROW = 144, RED_SZ = 4 * RED_ROW;
constexpr int LDS_BYTES = 112 * 1024;
static_assert(L_RED + 2 * RED_SZ <= LDS_BYTES, "lds");
constexpr int TW = 3;
constexpr unsigned SPIN_CAP = 1u << 17;

constexpr int DL[16] = {  105,   290,   420,   555,   675,   825,   970,   1090,
                          3,   1220,   1,   340,   20,
                          15,   4,   40};

constexpr int NOM_G = 85, NOM_Q = 92, NOM_O = 120;
#define SCHED() __builtin_amdgcn_sched_barrier(0)
#define STAMP(i) do {} while (0)
#define PASS(i) do {} while (0)
#define STAMP_FLUSH() do {} while (0)
#define DELAY(i) do { if (DL[i] > 0) __builtin_amdgcn_s_sleep(DL[i]); } while (0)
#define DLV(i) DL[i]
__device__ __forceinline__ unsigned rtc() { return (unsigned)__builtin_amdgcn_s_memrealtime(); }
#define WAIT_AT(ref, i) do { SCHED(); unsigned tg_ = (ref) + (unsigned)DLV(i); const unsigned lo_ = rtc() + (unsigned)DLV(13); if ((int)(lo_ - tg_) > 0) tg_ = lo_; \
    for (int k_ = 0; k_ < 4096 && (int)(rtc() - tg_) < 0; ++k_) {} SCHED(); } while (0)
#define PW_A 0
#define VPRE 0
#define DN_LATE 1
#define PW_B 1
#define UNLIKELY(x) __builtin_expect(!!(x), 0)
#define LIKELY(x) __builtin_expect(!!(x), 1)
#define PIN(v) asm volatile("" : "+v"(v))

__device__ __forceinline__ float bflo(unsigned u) { return __uint_as_float(u << 16); }
__device__ __forceinline__ float bfhi(unsigned u) { return __uint_as_float(u & 0xffff0000u); }
__device__ __forceinline__ unsigned pk(float lo, float hi) { bf16x2 p = {(__bf16)lo, (__bf16)hi}; return __builtin_bit_cast(unsigned, p); }
__device__ __forceinline__ float rbf(float f) { return bflo((unsigned)__builtin_bit_cast(u16, (__bf16)f)); }
__device__ __forceinline__ float vmax(float a, float b) { return __builtin_elementwise_maximum(a, b); }
template <int CTRL> __device__ __forceinline__ float dpp(float x) {
  return __builtin_bit_cast(float, __builtin_amdgcn_mov_dpp(__builtin_bit_cast(int, x), CTRL, 0xf, 0xf, true));
}
template <int CTRL> __device__ __forceinline__ unsigned dppu(unsigned x) { return (unsigned)__builtin_amdgcn_mov_dpp((int)x, CTRL, 0xf, 0xf, true); }
constexpr int XOR1 = 0xB1, XOR2 = 0x4E, HMIR = 0x141, MIR = 0x140, ROR4 = 0x124, ROR8 = 0x128;
__device__ __forceinline__ float row_sum(float v) { v += dpp<XOR1>(v); v += dpp<XOR2>(v); v += dpp<HMIR>(v); v += dpp<MIR>(v); return v; }
__device__ __forceinline__ float row_max(float v) { v = vmax(v, dpp<XOR1>(v)); v = vmax(v, dpp<XOR2>(v)); v = vmax(v, dpp<HMIR>(v)); v = vmax(v, dpp<MIR>(v)); return v; }
__device__ __forceinline__ float xrow_sum(float x) {
  auto s = __builtin_amdgcn_permlane16_swap(__float_as_uint(x), __float_as_uint(x), false, false);
  x = __uint_as_float(s[0]) + __uint_as_float(s[1]);
  auto t = __builtin_amdgcn_permlane32_swap(__float_as_uint(x), __float_as_uint(x), false, false);
  return __uint_as_float(t[0]) + __uint_as_float(t[1]);
}
__device__ __forceinline__ float xrow_max(float x) {
  auto s = __builtin_amdgcn_permlane16_swap(__float_as_uint(x), __float_as_uint(x), false, false);
  x = vmax(__uint_as_float(s[0]), __uint_as_float(s[1]));
  auto t = __builtin_amdgcn_permlane32_swap(__float_as_uint(x), __float_as_uint(x), false, false);
  return vmax(__uint_as_float(t[0]), __uint_as_float(t[1]));
}
__device__ __forceinline__ float rows_reduce4(f32x4 v) {
  auto a = __builtin_amdgcn_permlane16_swap(__float_as_uint(v[0]), __float_as_uint(v[1]), false, false);
  auto b = __builtin_amdgcn_permlane16_swap(__float_as_uint(v[2]), __float_as_uint(v[3]), false, false);
  const float t01 = __uint_as_float(a[0]) + __uint_as_float(a[1]), t23 = __uint_as_float(b[0]) + __uint_as_float(b[1]);
  auto c = __builtin_amdgcn_permlane32_swap(__float_as_uint(t01), __float_as_uint(t23), false, false);
  return __uint_as_float(c[0]) + __uint_as_float(c[1]);
}
__device__ __forceinline__ f32x4 mfma32(v4u a, v4u b, f32x4 c) {
  return __builtin_amdgcn_mfma_f32_16x16x32_bf16(__builtin_bit_cast(bf16x8, a), __builtin_bit_cast(bf16x8, b), c, 0, 0, 0);
}
__device__ __forceinline__ f32x4 mfma16(v2u a, v2u b, f32x4 c) {
  return __builtin_amdgcn_mfma_f32_16x16x16bf16_1k(__builtin_bit_cast(s16x4, a), __builtin_bit_cast(s16x4, b), c, 0, 0, 0);
}
__device__ __forceinline__ float dot2(unsigned a, unsigned b, float c) {
  return __builtin_amdgcn_fdot2_f32_bf16(__builtin_bit_cast(bf16x2, a), __builtin_bit_cast(bf16x2, b), c, false);
}
__device__ __forceinline__ v2u trread(LAS unsigned char* p) { return __builtin_bit_cast(v2u, __builtin_amdgcn_ds_read_tr16_b64_v4i16((LAS s16x4*)p)); }
__device__ __forceinline__ v4u ldg_nt(const u16* p) { return __builtin_nontemporal_load((const GAS v4u*)p); }
__device__ __forceinline__ v4u ld_sc1(__amdgpu_buffer_rsrc_t rs, int off) { return __builtin_amdgcn_raw_buffer_load_b128(rs, off, 0, 16); }
__device__ __forceinline__ unsigned ld1_sc1(__amdgpu_buffer_rsrc_t rs, int off) { return __builtin_amdgcn_raw_buffer_load_b32(rs, off, 0, 16); }
__device__ __forceinline__ v2u ld2_sc1(__amdgpu_buffer_rsrc_t rs, int off) { return __builtin_amdgcn_raw_buffer_load_b64(rs, off, 0, 16); }
__device__ __forceinline__ void st2_sc1(__amdgpu_buffer_rsrc_t rs, int off, v2u v) { __builtin_amdgcn_raw_buffer_store_b64(v, rs, off, 0, 16); }
__device__ __forceinline__ void st_sc1(__amdgpu_buffer_rsrc_t rs, int off, v4u v) { __builtin_amdgcn_raw_buffer_store_b128(v, rs, off, 0, 16); }
__device__ __forceinline__ void st1_sc1(__amdgpu_buffer_rsrc_t rs, int off, unsigned v) { __builtin_amdgcn_raw_buffer_store_b32(v, rs, off, 0, 16); }
#define PERM(hi, lo, sel) __builtin_amdgcn_perm((hi), (lo), (sel))
__device__ __forceinline__ bool okH(v4u d, unsigned T) { return (PERM(d.y, d.x, 0x05040100u) == T) & (PERM(d.w, d.z, 0x05040100u) == T); }
__device__ __forceinline__ v2u valH(v4u d) { return v2u{PERM(d.y, d.x, 0x07060302u), PERM(d.w, d.z, 0x07060302u)}; }
__device__ __forceinline__ v4u mkH(unsigned p0, unsigned p1, unsigned T) {
  return v4u{PERM(p0, T, 0x05040100u), PERM(p0, T, 0x07060302u), PERM(p1, T, 0x05040100u), PERM(p1, T, 0x07060302u)};
}
__device__ __forceinline__ v4u unF(v4u d, unsigned T) { return v4u{d.x ^ (T & 0xffu), d.y ^ ((T >> 8) & 0xffu), d.z ^ ((T >> 16) & 0xffu), d.w ^ (T >> 24)}; }
__device__ __forceinline__ unsigned or4(v4u d) { return (d.x | d.y) | (d.z | d.w); }
__device__ __forceinline__ v4u mkF(float a, float b, float c, float d, unsigned T) {
  return v4u{PERM(__float_as_uint(a), T, 0x07060500u), PERM(__float_as_uint(b), T, 0x07060501u),
             PERM(__float_as_uint(c), T, 0x07060502u), PERM(__float_as_uint(d), T, 0x07060503u)};
}
#define LGKM0() asm volatile("s_waitcnt lgkmcnt(0)" ::: "memory")
#define BARRIER() do { LGKM0(); __builtin_amdgcn_s_barrier(); asm volatile("" ::: "memory"); } while (0)

__device__ __forceinline__ float gemv128(v2u a0, v2u a1, const v2u (&B)[8]) {
  const f32x4 z = {0.f, 0.f, 0.f, 0.f};
  f32x4 c0 = mfma16(a0, B[0], z), c1 = mfma16(a0, B[1], z), c2 = mfma16(a0, B[2], z), c3 = mfma16(a0, B[3], z);
  c0 = mfma16(a1, B[4], c0); c1 = mfma16(a1, B[5], c1); c2 = mfma16(a1, B[6], c2); c3 = mfma16(a1, B[7], c3);
  return (c0[0] + c1[1]) + (c2[2] + c3[3]);
}
struct Ctx {
  const u16 *vc, *wo, *wg, *wu, *wd; u16* out; unsigned char* ws;
  __amdgpu_buffer_rsrc_t rs; LAS unsigned char* L;
  int lane, w, wv, kg, i16, b, g, c; unsigned E, T, t0, tref;
};

#define LPOST(set) do { asm volatile("" ::: "memory"); *(volatile LAS unsigned*)(L + L_FL + (set) * 16 + w * 4) = T; asm volatile("" ::: "memory"); } while (0)
#define LWAIT4(set) do { for (unsigned spins_ = 0; spins_ < (1u << 24); ++spins_) { const v4u f_ = *(volatile LAS v4u*)(L + L_FL + (set) * 16); \
      if ((f_.x == T) & (f_.y == T) & (f_.z == T) & (f_.w == T)) break; } asm volatile("" ::: "memory"); } while (0)
#define LWAIT1(set, who) do { for (unsigned spins_ = 0; spins_ < (1u << 24); ++spins_) { \
      if (*(volatile LAS unsigned*)(L + L_FL + (set) * 16 + (who) * 4) == T) break; __builtin_amdgcn_s_sleep(1); } asm volatile("" ::: "memory"); } while (0)
#define ONES 0x3f803f80u
#define FL4(set) (*(volatile LAS v4u*)(L + L_FL + (set) * 16))
#define FL4OK(f) (((f).x == T) & ((f).y == T) & ((f).z == T) & ((f).w == T))

__device__ __forceinline__ v4u load_sw(const Ctx& X, int n) {
  const int cb = (X.c & 7) < 7 ? (X.c & 7) : 6;
  if (n < 4) return ldg_nt(X.wo + (size_t)(512 * X.g + 256 * (X.c & 1) + 64 * X.w + 16 * n + (X.lane >> 2)) * DM + 32 * (X.c >> 1) + 8 * (X.lane & 3));
  if (n < 8) return ldg_nt((n >= 6 ? X.wu : X.wg) + (size_t)(128 * (X.c >> 3) + 32 * X.w + 16 * (n & 1) + (X.lane >> 2)) * FF + 224 * X.g + 32 * cb + 8 * (X.lane & 3));
  const int blk = 2 * X.w + (n & 1) < 7 ? 2 * X.w + (n & 1) : 6;
  return ldg_nt(X.wd + (size_t)(224 * X.g + 32 * blk + (X.lane >> 1)) * DM + 16 * X.c + 8 * (X.lane & 1));
}
__device__ __forceinline__ void run_A(Ctx& X, v4u (&Kf)[8], v4u (&Vf)[8], v4u (&Sw)[10]) {
  const int lane = X.lane, w = X.w, kg = X.kg, i16 = X.i16, g = X.g, c = X.c;
  const __amdgpu_buffer_rsrc_t rs = X.rs; LAS unsigned char* L = X.L; LAS unsigned char* stg = L + L_STG + w * STG_W;
  const f32x4 z = {0.f, 0.f, 0.f, 0.f};
  STAMP(12);
  const int u = (lane * 43) >> 7;
  const bool head3 = lane == 3 * u;
  int ooff = head3 ? WS_GRP + g * GRP_SZ + GO_O + w * 12288 + u * 512 + c * 16 : (lane == 1 ? WS_GRP + g * GRP_SZ + GO_ML + w * 512 + c * 16 : OOB);
  const int ostep = (head3 || lane == 1) ? O_L : 0;
  const u16* vb = X.vc + ((size_t)g * SK + (SK - WIN) + 128 * c + 32 * w) * DH;
  {
#pragma unroll
    for (int s = 0; s < 8; ++s) {
      *(LAS v4u*)(stg + (4 * s + kg) * 272 + i16 * 16) = Kf[s];
      SCHED();
      if (s + VPRE < 8) { const int j = s + VPRE; Vf[j] = ldg_nt(vb + (16 * (j >> 2) + 4 * kg + (j & 3)) * DH + 8 * i16); }
      SCHED();
    }
    LGKM0();
#pragma unroll
    for (int s = 0; s < 8; ++s) Kf[s] = *(LAS v4u*)(stg + (16 * (s >> 2) + i16) * 272 + (s & 3) * 64 + kg * 16);
  }
  {
    v4u t[8];
#pragma unroll
    for (int jj = 0; jj < 4; ++jj) {
#pragma unroll
      for (int n = 0; n < 8; ++n) t[n][jj] = PERM(Vf[2 * jj + 1][n >> 1], Vf[2 * jj][n >> 1], (n & 1) ? 0x07060302u : 0x05040100u);
      asm volatile("" : "+v"(t[0][jj]), "+v"(t[7][jj]));
      SCHED();
      Sw[jj] = load_sw(X, jj);
      SCHED();
    }
#pragma unroll
    for (int n = 0; n < 8; ++n) Vf[n] = t[n];
    asm volatile("" :: "v"(Vf[0]), "v"(Vf[1]), "v"(Vf[2]), "v"(Vf[3]), "v"(Vf[4]), "v"(Vf[5]), "v"(Vf[6]), "v"(Vf[7]));
  }
  STAMP(13);
#pragma unroll
  for (int n = 0; n < 4; ++n) { *(LAS v4u*)(L + L_IWO + w * 4096 + n * 1024 + lane * 16) = Sw[n]; SCHED(); Sw[4 + n] = load_sw(X, 4 + n); SCHED(); }
#pragma unroll
  for (int n = 0; n < 4; ++n) {
    *(LAS v4u*)(L + L_IGU + (n >> 1) * 8192 + w * 2048 + (n & 1) * 1024 + lane * 16) = Sw[4 + n]; SCHED();
    if (n & 1) { Sw[8 + (n >> 1)] = load_sw(X, 8 + (n >> 1)); SCHED(); }
  }
  if (DN_LATE == 0) {
#pragma unroll
    for (int n = 0; n < 2; ++n) *(LAS v4u*)(L + L_IDN + (2 * w + n) * 1024 + lane * 16) = Sw[8 + n];
  }
  STAMP(9);
#pragma unroll
  for (int layer = 0; layer < 2; ++layer) {
    BARRIER();
    const unsigned T = __builtin_amdgcn_readfirstlane(*(volatile LAS unsigned*)(L + L_FL));
    {
      v4u qf[4];
#pragma unroll
      for (int s = 0; s < 4; ++s) qf[s] = *(LAS v4u*)(L + L_QB + (i16 >> 2) * QSTR + s * 64 + kg * 16);
      f32x4 sa = z, sb = z;
#pragma unroll
      for (int s = 0; s < 4; ++s) { sa = mfma32(Kf[s], qf[s], sa); sb = mfma32(Kf[4 + s], qf[s], sb); }
      float sv[8];
      if (layer == 0) {
#pragma unroll
        for (int k = 0; k < 8; k += 2) {
          const unsigned p = k < 4 ? pk(sa[k & 3], sa[(k & 3) + 1]) : pk(sb[k & 3], sb[(k & 3) + 1]);
          const unsigned r = pk(bflo(p) * 0.08838834764831845f, bfhi(p) * 0.08838834764831845f);
          sv[k] = bflo(r); sv[k + 1] = bfhi(r);
        }
      } else {
#pragma unroll
        for (int k = 0; k < 4; ++k) { sv[k] = sa[k]; sv[4 + k] = sb[k]; }
      }
      const float CE = layer == 0 ? 1.4426950408889634f : 1.4426950408889634f * 0.08838834764831845f;
      const float m = xrow_max(vmax(vmax(vmax(sv[0], sv[1]), vmax(sv[2], sv[3])), vmax(vmax(sv[4], sv[5]), vmax(sv[6], sv[7])))) * CE;
#pragma unroll
      for (int k = 0; k < 8; ++k) sv[k] = __builtin_amdgcn_exp2f(fmaf(sv[k], CE, -m));
      const v4u pf = {pk(sv[0], sv[1]), pk(sv[2], sv[3]), pk(sv[4], sv[5]), pk(sv[6], sv[7])};
      f32x4 o[8];
#pragma unroll
      for (int n = 0; n < 8; ++n) o[n] = mfma32(pf, Vf[n], z);
      const f32x4 lv = mfma32(pf, v4u{ONES, ONES, ONES, ONES}, z);
      LAS unsigned char* op = L + L_ATT + w * 2048 + kg * 512 + i16 * 32;
      *(LAS f32x4*)op = f32x4{o[0][0], o[1][1], o[2][2], o[3][3]};
      *(LAS f32x4*)(op + 16) = f32x4{o[4][0], o[5][1], o[6][2], o[7][3]};
      if (i16 == 4 * kg) *(LAS f32x2*)(L + L_ML + kg * 32 + w * 8) = f32x2{m, lv[0]};
    }
    LPOST(1 + layer);
    const unsigned tm = rtc();
    {
      f32x4 ml0, ml1; f32x2 ov[4];
      for (unsigned sp = 0; sp < (1u << 24); ++sp) {
        const v4u f = FL4(1 + layer);
        ml0 = *(volatile LAS f32x4*)(L + L_ML + w * 32); ml1 = *(volatile LAS f32x4*)(L + L_ML + w * 32 + 16);
#pragma unroll
        for (int k = 0; k < 4; ++k) ov[k] = *(volatile LAS f32x2*)(L + L_ATT + k * 2048 + w * 512 + lane * 8);
        if (FL4OK(f)) break;
      }
      const float M = vmax(vmax(ml0[0], ml0[2]), vmax(ml1[0], ml1[2]));
      const float e0 = __builtin_amdgcn_exp2f(ml0[0] - M), e1 = __builtin_amdgcn_exp2f(ml0[2] - M), e2 = __builtin_amdgcn_exp2f(ml1[0] - M), e3 = __builtin_amdgcn_exp2f(ml1[2] - M);
      const f32x2 a = (ov[0] * e0 + ov[1] * e1) + (ov[2] * e2 + ov[3] * e3);
      const float ls = fmaf(e0, ml0[1], e1 * ml0[3]) + fmaf(e2, ml1[1], e3 * ml1[3]);
      unsigned dv = pk(a[0], a[1]);
      unsigned d1 = dppu<0x130>(dv);
      unsigned d2 = dppu<0x130>(d1);
      dv = head3 ? dv : __float_as_uint(M); d1 = head3 ? d1 : __float_as_uint(ls); d2 = head3 ? d2 : tm;
      st_sc1(rs, ooff, v4u{dv, d1, d2, T});
      ooff += ostep; asm volatile("" : "+v"(ooff));
    }
    if (layer == 0) { STAMP(3);
      if (DN_LATE) {
#pragma unroll
        for (int n = 0; n < 2; ++n) *(LAS v4u*)(L + L_IDN + (2 * w + n) * 1024 + lane * 16) = Sw[8 + n];
      }
      LPOST(15);
    } else STAMP(15);
  }
}

#define RETRY(pi, reload) if (LIKELY(__all(ok)) || spins > SPIN_CAP) break; asm volatile("" ::: "memory"); DELAY(8); PASS(pi); if (!ok) { reload; }
#define SLIP(i, lat) do { const int late_ = (int)(rtc() - (X.tref + (unsigned)DLV(i) + (unsigned)(lat))); if (late_ > 0) X.tref += (unsigned)(late_ + DLV(15)); } while (0)
#define RESYNC(tmax, i, nom) do { const int late_ = (int)((tmax) - (X.tref + (unsigned)DLV(i) + (unsigned)(nom))); if (late_ > 0) X.tref += (unsigned)late_; } while (0)
#define QUAD_TMAX(tv) ({ int dt_ = (int)((tv) - X.tref); dt_ = max(dt_, (int)dppu<XOR1>((unsigned)dt_)); dt_ = max(dt_, (int)dppu<XOR2>((unsigned)dt_)); X.tref + (unsigned)__builtin_amdgcn_readfirstlane(dt_); })
#define LN_STATS(ds) float s1 = __uint_as_float((ds).x), s2 = __uint_as_float((ds).y); \
      s1 += dpp<XOR1>(s1); s2 += dpp<XOR1>(s2); s1 += dpp<XOR2>(s1); s2 += dpp<XOR2>(s2); \
      const float mean = s1 * (1.f / DM), rstd = __builtin_amdgcn_rsqf(fmaxf(fmaf(-mean, mean, s2 * (1.f / DM)), 0.f) + 1e-5f)

template <int layer>
__device__ __forceinline__ void run_B(Ctx& X, const v2u (&Bq)[8], v4u (&Aw)[12], v2u (&Bg)[8], v4u (&Bd)[7], float& xr, f32x4& csq, float (&csg)[2]) {
  const int lane = X.lane, w = X.w, kg = X.kg, i16 = X.i16, g = X.g, c = X.c, kq = c >> 3;
  const unsigned T = X.T; const __amdgpu_buffer_rsrc_t rs = X.rs; LAS unsigned char* L = X.L;
  LAS unsigned char* red = L + L_RED + layer * RED_SZ;
  const f32x4 z = {0.f, 0.f, 0.f, 0.f};
  const int grp = WS_GRP + g * GRP_SZ;
  v2u fy0; v4u c0, c1; unsigned tq;
  const int qun = kg * 8 + (lane & 7), qidx = ((lane >> 3) & 1) * 2048 + (32 * w + qun) * 16;
  const int xun = 8 * w + (lane & 7);
  {
    int qoff = (layer == 0 ? WS_Q0 + g * 8192 : grp + GO_Q1) + qidx, soff = grp + GO_ST2 + (lane & 3) * 16;
    PIN(qoff); PIN(soff);
    if (layer == 0) WAIT_AT(X.t0, 11); else WAIT_AT(X.tref, 4);
    v4u ds = v4u{0u, 0u, 0u, T}, r0, r1;
#define QLOADS() do { if (layer == 1) ds = ld_sc1(rs, soff); r0 = ld_sc1(rs, qoff); r1 = ld_sc1(rs, qoff + 4096); } while (0)
    QLOADS();
    SCHED();
    for (unsigned spins = 0;; ++spins) {
      LN_STATS(ds);
      const v4u d0 = unF(r0, T), d1 = unF(r1, T);
      float qv[4];
#pragma unroll
      for (int k = 0; k < 4; ++k) {
        float t = __uint_as_float(d0[k]) + __uint_as_float(d1[k]); t += dpp<ROR8>(t);
        qv[k] = layer == 0 ? t : rstd * fmaf(-mean, csq[k], t);
      }
      *(LAS v2u*)(L + L_QB + w * QSTR + qun * 8) = v2u{pk(qv[0], qv[1]), pk(qv[2], qv[3])};
      const bool ok = (((or4(d0) | or4(d1)) & 0xffu) == 0u) & (ds.w == T);
      RETRY(layer ? 7 : 0, QLOADS());
    }
    tq = ds.z;
    STAMP(2 + 12 * layer);
  }
  BARRIER();
  if (layer == 1) { RESYNC(QUAD_TMAX(tq), 3, NOM_Q); SLIP(4, 72); }
  if (layer == 0) {
    tq = rtc();
    if (w == TW && lane == 0) st2_sc1(rs, WS_FO + (g * 32 + c) * 128, v2u{T, tq});
    fy0 = ld2_sc1(rs, w == TW ? WS_FO + (g * 32 + (lane & 31)) * 128 : OOB);
    c0 = ld_sc1(rs, WS_Q0 + 65536 + g * 8192 + qidx); c1 = ld_sc1(rs, WS_Q0 + 65536 + g * 8192 + qidx + 4096);
    SCHED();
  }
#define WO_PREP() do { const int hh = w >> 1; \
_Pragma("unroll") \
    for (int t = 0; t < 6; ++t) { \
      const int un = 4 * t + kg, r0 = 6 * un + (i16 >> 2); \
      const int ra = r0 > 127 ? 127 : r0, rb = r0 + 4 > 127 ? 127 : r0 + 4; \
_Pragma("unroll") \
      for (int ct = 0; ct < 2; ++ct) { \
        const v2u lo = trread(L + L_IWO + (hh * 128 + ra) * 64 + ct * 32 + (i16 & 3) * 8), hi = trread(L + L_IWO + (hh * 128 + rb) * 64 + ct * 32 + (i16 & 3) * 8); \
        Aw[t * 2 + ct] = v4u{(t == 5 && un > 21) ? 0u : lo.x, (t == 5 && un > 20) ? 0u : lo.y, (t == 5 && un > 20) ? 0u : hi.x, 0u}; \
      } \
    } \
  } while (0)
  if (layer == 0) {
    WO_PREP();
#pragma unroll
    for (int u = 0; u < 8; ++u) Bg[u] = trread(L + L_IGU + (w >> 1) * 8192 + (64 * (u >> 2) + 16 * kg + 4 * (u & 3) + (i16 >> 2)) * 64 + (w & 1) * 32 + (i16 & 3) * 8);
    const v2u one = {ONES, ONES};
    const float cs = gemv128(one, one, Bg);
    const float p1 = dpp<0x55>(cs), p2 = dpp<0xAA>(cs), p3 = dpp<0xFF>(cs);
    st_sc1(rs, ((lane & 0x33) == 0 && (c & 7) < 7) ? WS_GC + g * 8192 + kq * 2048 + (w >> 1) * 1024 + (8 * (c & 7) + 4 * (w & 1) + (i16 >> 2)) * 16 : OOB, mkF(cs, p1, p2, p3, T));

  }
  {
    const int hh = w >> 1, st = w & 1, h = 2 * (c & 1) + hh;
    int obase = grp + GO_O + layer * O_L + h * 12288 + (16 * st + i16) * 16 + kg * 512;
    int o5 = obase + 5 * 2048 - (kg & 2) * 512;
    const int rot = (4 * kg + i16) & 15;
    int mlA = grp + GO_ML + layer * O_L + h * 512 + (16 * st + rot) * 16, mlB = mlA ^ 256;
    const int slot = ((lane >> 4) & 1) * 4 + (lane & 3), rep = ((lane >> 2) & 3) + 4 * (lane >> 5);
    int yst = WS_Y + layer * 262144 + rep * 32768 + (2 * g + (c & 1)) * 2048 + (c >> 1) * 128 + slot * 16;
    PIN(obase); PIN(o5); PIN(mlA); PIN(mlB); PIN(yst);
    if (layer == 0) {
      unsigned tg;
      if (w == TW) {
        for (unsigned spins = 0;; ++spins) {
          if ((lane & 31) == c) fy0 = v2u{T, tq};
          if (__all(fy0.x == T) || spins > SPIN_CAP) break;
          asm volatile("" ::: "memory"); DELAY(10); PASS(1);
          fy0 = ld2_sc1(rs, WS_FO + (g * 32 + (lane & 31)) * 128);
        }
        int dt = (int)(fy0.y - X.t0); dt = max(dt, (int)dppu<XOR1>((unsigned)dt)); dt = max(dt, (int)dppu<XOR2>((unsigned)dt)); dt = max(dt, (int)dppu<HMIR>((unsigned)dt));
        dt = max(dt, (int)dppu<MIR>((unsigned)dt)); dt = max(__builtin_amdgcn_readlane(dt, 0), __builtin_amdgcn_readlane(dt, 16)); tg = X.t0 + (unsigned)dt;
        *(volatile LAS unsigned*)(L + L_FL + 3 * 16) = tg; LPOST(3);
      } else { LWAIT1(3, TW); tg = __builtin_amdgcn_readfirstlane(*(volatile LAS unsigned*)(L + L_FL + 3 * 16)); }
      if (c == 0 && w == TW && lane < 8) st2_sc1(rs, WS_FY + (lane * 8 + g) * 128, v2u{T, tg});
      for (unsigned spins = 0;; ++spins) {
        c0 = unF(c0, T); c1 = unF(c1, T);
        if (__all(((or4(c0) | or4(c1)) & 0xffu) == 0u) || spins > SPIN_CAP) break;
        asm volatile("" ::: "memory"); DELAY(8); c0 = ld_sc1(rs, WS_Q0 + 65536 + g * 8192 + qidx); c1 = ld_sc1(rs, WS_Q0 + 65536 + g * 8192 + qidx + 4096);
      }
#pragma unroll
      for (int k = 0; k < 4; ++k) { float t = __uint_as_float(c0[k]) + __uint_as_float(c1[k]); t += dpp<ROR8>(t); csq[k] = t; }
      { unsigned t_ = tg + (unsigned)DLV(0); for (int k_ = 0; k_ < 4096 && (int)(rtc() - t_) < 0; ++k_) {} }
      X.tref = tg;
    } else WAIT_AT(X.tref, 5);
    if (layer == 0) fy0 = ld2_sc1(rs, WS_FY + (g * 8 + (lane & 7)) * 128);
    v4u d[8];
#define OLOADS() do { d[6] = ld_sc1(rs, mlA); d[7] = ld_sc1(rs, mlB); _Pragma("unroll") for (int t = 0; t < 5; ++t) d[t] = ld_sc1(rs, obase + t * 2048); d[5] = ld_sc1(rs, o5); } while (0)
    OLOADS();
    SCHED();
    for (unsigned spins = 0;; ++spins) {
      const float mA = __uint_as_float(d[6].x), lA = __uint_as_float(d[6].y), mB = __uint_as_float(d[7].x), lB = __uint_as_float(d[7].y);
      const float M = row_max(vmax(mA, mB));
      const float eA = __builtin_amdgcn_exp2f(mA - M), eB = __builtin_amdgcn_exp2f(mB - M);
      const float Ls = row_sum(fmaf(eA, lA, eB * lB));
      const float wgt = eA * __builtin_amdgcn_rcpf(Ls);
      const float w0 = dpp<0x150>(wgt), w1 = dpp<0x151>(wgt), w2 = dpp<0x152>(wgt), w3 = dpp<0x153>(wgt);
      f32x4 a0 = z, a1 = z;
#pragma unroll
      for (int t = 0; t < 6; ++t) { a0 = mfma32(d[t], Aw[2 * t], a0); a1 = mfma32(d[t], Aw[2 * t + 1], a1); }
      const float y0 = fmaf(a0[0], w0, a0[1] * w1) + fmaf(a0[2], w2, a0[3] * w3), y1 = fmaf(a1[0], w0, a1[1] * w1) + fmaf(a1[2], w2, a1[3] * w3);
      auto sw = __builtin_amdgcn_permlane16_swap(__float_as_uint(y0), __float_as_uint(y1), false, false);
      const float yy = __uint_as_float(sw[0]) + __uint_as_float(sw[1]);
      auto sx = __builtin_amdgcn_permlane32_swap(__float_as_uint(yy), __float_as_uint(yy), false, false);
      *(LAS float*)(red + w * RED_ROW + (lane & 31) * 4) = __uint_as_float(sx[0]) + __uint_as_float(sx[1]);
      const bool ok = (d[0].w == T) & (d[1].w == T) & (d[2].w == T) & (d[3].w == T) & (d[4].w == T) & (d[5].w == T) & (d[6].w == T) & (d[7].w == T);
      RETRY(layer ? 8 : 1, OLOADS());
    }
    if (layer == 0) asm volatile("s_waitcnt vmcnt(0)" ::: "memory");
    LPOST(4 + 5 * layer);
    STAMP(4 + 12 * layer);
    if (w == TW) {
      f32x4 v;
      for (unsigned sp = 0; sp < (1u << 24); ++sp) {
        const v4u f = FL4(4 + 5 * layer);
        v = *(volatile LAS f32x4*)(red + ((lane >> 2) & 3) * RED_ROW + slot * 16);
        if (FL4OK(f)) break;
      }
#pragma unroll
      for (int k = 0; k < 4; ++k) { float t = v[k]; t += dpp<ROR8>(t); t += dpp<ROR4>(t); v[k] = t; }
      st_sc1(rs, yst, mkF(v[0], v[1], v[2], v[3], T));
    }
    if (layer == 1) {
      int dt = max((int)(d[6].z - X.tref), (int)(d[7].z - X.tref));
      dt = max(dt, (int)dppu<XOR1>((unsigned)dt)); dt = max(dt, (int)dppu<XOR2>((unsigned)dt)); dt = max(dt, (int)dppu<HMIR>((unsigned)dt)); dt = max(dt, (int)dppu<MIR>((unsigned)dt));
      RESYNC(X.tref + (unsigned)__builtin_amdgcn_readfirstlane(dt), 4, NOM_O);
      SLIP(5, 125);
    }
  }
  STAMP(5 + 12 * layer);
#define X_SHARE(xb, setx, sets) do { LPOST(setx); \
    float s1 = xr, s2 = xr * xr; \
    s1 += dpp<XOR1>(s1); s2 += dpp<XOR1>(s2); s1 += dpp<XOR2>(s1); s2 += dpp<XOR2>(s2); s1 += dpp<HMIR>(s1); s2 += dpp<HMIR>(s2); \
    *(LAS f32x2*)((xb) + 256 + (4 * w + kg) * 8) = f32x2{s1, s2}; LPOST(sets); } while (0)
#define X_FETCH(xb, setx) v2u a0, a1; for (unsigned sp = 0; sp < (1u << 24); ++sp) { const v4u f = FL4(setx); \
      a0 = *(volatile LAS v2u*)((xb) + (4 * kg + (i16 & 3)) * 8); a1 = *(volatile LAS v2u*)((xb) + 128 + (4 * kg + (i16 & 3)) * 8); if (FL4OK(f)) break; }
#define STATS_PUBLISH(xb, sets, idx) do { f32x2 sp2; for (unsigned sp = 0; sp < (1u << 24); ++sp) { const v4u f = FL4(sets); sp2 = *(volatile LAS f32x2*)((xb) + 256 + i16 * 8); if (FL4OK(f)) break; } \
    const float S1 = row_sum(sp2[0]), S2 = row_sum(sp2[1]); \
    st_sc1(rs, lane == 0 ? grp + (idx) + kq * 16 : OOB, v4u{__float_as_uint(S1), __float_as_uint(S2), rtc(), T}); } while (0)
  {
    const int go = kq * 2048 + (w >> 1) * 1024 + (8 * (c & 7) + 4 * (w & 1) + (i16 >> 2)) * 16;
    const bool gst = (lane & 0x33) == 0 && (c & 7) < 7;
    int goff = gst ? grp + GO_G + layer * O_L + go : OOB;
    int yoff = WS_Y + layer * 262144 + g * 32768 + (lane >> 3) * 2048 + (32 * kq + xun) * 16;
    PIN(goff); PIN(yoff);
    if (layer == 0) {
      {
        for (unsigned spins = 0;; ++spins) {
          if ((lane & 7) == g) fy0 = v2u{T, X.tref};
          if (__all(fy0.x == T) || spins > SPIN_CAP) break;
          asm volatile("" ::: "memory"); DELAY(14); PASS(3);
          fy0 = ld2_sc1(rs, WS_FY + (g * 8 + (lane & 7)) * 128);
        }
        int dt = (int)(fy0.y - X.t0); dt = max(dt, (int)dppu<XOR1>((unsigned)dt)); dt = max(dt, (int)dppu<XOR2>((unsigned)dt)); dt = max(dt, (int)dppu<HMIR>((unsigned)dt));
        X.tref = X.t0 + (unsigned)__builtin_amdgcn_readfirstlane(dt);
      }
      { unsigned t_ = X.tref + (unsigned)DLV(1); for (int k_ = 0; k_ < 4096 && (int)(rtc() - t_) < 0; ++k_) {} }
    } else WAIT_AT(X.tref, 6);
    LAS unsigned char* xb = L + L_XB + 2 * layer * XB_SZ;
    float xn;
    v4u r0 = ld_sc1(rs, yoff), r1 = ld_sc1(rs, yoff + 16384);
    SCHED();
    for (unsigned spins = 0;; ++spins) {
      const v4u d0 = unF(r0, T), d1 = unF(r1, T);
      float t = rows_reduce4(__builtin_bit_cast(f32x4, d0) + __builtin_bit_cast(f32x4, d1));
      t += dpp<ROR8>(t);
      const unsigned p = pk(xr + t, 0.f);
      *(LAS u16*)(xb + (4 * xun + kg) * 2) = (u16)p;
      xn = bflo(p);
      const bool ok = ((or4(d0) | or4(d1)) & 0xffu) == 0u;
      RETRY(layer ? 9 : 4, r0 = ld_sc1(rs, yoff); r1 = ld_sc1(rs, yoff + 16384));
    }
    xr = xn;
    STAMP(6 + 12 * layer);
    X_SHARE(xb, 6 + 4 * layer, 12 + layer);
    if ((c & 7) < 7) {
      X_FETCH(xb, 6 + 4 * layer);
      const float part = gemv128(a0, a1, Bg);
      const float p1 = dpp<0x55>(part), p2 = dpp<0xAA>(part), p3 = dpp<0xFF>(part);
      st_sc1(rs, goff, mkF(part, p1, p2, p3, T));
    } else if (w == 0) STATS_PUBLISH(xb, 12 + layer, GO_ST + layer * O_L);
  }
  if (layer == 1) STAMP(19);
  SLIP(layer == 0 ? 1 : 6, 98);
  if (layer == 0) {
    const v4u zz = {0u, 0u, 0u, 0u};
    const int slot = ((lane >> 4) & 1) * 4 + (lane & 3), rep = ((lane >> 2) & 3) + 4 * (lane >> 5);
    if (w == TW) {
      st_sc1(rs, WS_Y + 262144 + rep * 32768 + (2 * g + (c & 1)) * 2048 + (c >> 1) * 128 + slot * 16, zz);
      st_sc1(rs, i16 < 8 ? WS_X + 131072 + i16 * 16384 + g * 2048 + c * 64 + kg * 16 : OOB, zz);
    }
    st_sc1(rs, lane < 30 ? grp + O_L + (c * 4 + w) * 480 + lane * 16
                         : ((lane & 0x33) == 0x20 ? grp + GO_Q1 + kq * 2048 + ((c & 7) * 64 + w * 16 + i16) * 4 : ((lane == 63 && (c & 7) == 0 && w == 0) ? grp + GO_ST2 + kq * 16 : OOB)), zz);
  }
  {
    const int hu = 14 * w + (i16 < 14 ? i16 : 13);
    const int gi = kg * 2048 + hu * 16;
    int gld = grp + GO_G + layer * O_L + gi, soff = grp + GO_ST + layer * O_L + (lane & 3) * 16;
    int xst = i16 < 8 ? WS_X + layer * 131072 + i16 * 16384 + g * 2048 + c * 64 + kg * 16 : OOB;
    PIN(gld); PIN(soff); PIN(xst);
    if (layer == 0 && w == TW) {
      LWAIT4(15);
#pragma unroll
      for (int t = 0; t < 7; ++t) {
        const int r0 = 32 * t + 8 * kg + (i16 >> 2);
        const v2u lo = trread(L + L_IDN + r0 * 32 + (i16 & 3) * 8), hi = trread(L + L_IDN + (r0 + 4) * 32 + (i16 & 3) * 8);
        Bd[t] = v4u{lo.x, lo.y, hi.x, hi.y};
      }
    }
    WAIT_AT(X.tref, layer == 0 ? 2 : 7);
    LAS unsigned char* hb = L + L_HB + layer * 512;
    v4u ds, r0, r1;
#define GLOADS() do { ds = ld_sc1(rs, soff); if (layer == 0) { c0 = ld_sc1(rs, WS_GC + g * 8192 + gi); c1 = ld_sc1(rs, WS_GC + g * 8192 + gi + 1024); } \
      r0 = ld_sc1(rs, gld); r1 = ld_sc1(rs, gld + 1024); } while (0)
    GLOADS();
    SCHED();
    for (unsigned spins = 0;; ++spins) {
      LN_STATS(ds);
      unsigned cbad = 0u;
      if (layer == 0) {
        const v4u e0 = unF(c0, T), e1 = unF(c1, T); cbad = or4(e0) | or4(e1);
        csg[0] = rows_reduce4(__builtin_bit_cast(f32x4, e0)); csg[1] = rows_reduce4(__builtin_bit_cast(f32x4, e1));
      }
      const v4u d0 = unF(r0, T), d1 = unF(r1, T);
      const float gv = rstd * fmaf(-mean, csg[0], rows_reduce4(__builtin_bit_cast(f32x4, d0)));
      const float uv = rstd * fmaf(-mean, csg[1], rows_reduce4(__builtin_bit_cast(f32x4, d1)));
      const float hv = uv * __builtin_amdgcn_rcpf(1.f + __builtin_amdgcn_exp2f(-1.4426950408889634f * gv));
      *(LAS u16*)(hb + (4 * hu + kg) * 2) = (u16)pk(hv, 0.f);
      const bool ok = (((or4(d0) | or4(d1) | cbad) & 0xffu) == 0u) & (ds.w == T);
      RETRY(5, GLOADS());
    }
    if (layer == 0) asm volatile("s_waitcnt vmcnt(0)" ::: "memory");
    LPOST(7 + 4 * layer);
    STAMP(8 + 12 * layer);
    if (w == TW) {
      v4u hf[7];
      for (unsigned sp = 0; sp < (1u << 24); ++sp) {
        const v4u f = FL4(7 + 4 * layer);
#pragma unroll
        for (int t = 0; t < 7; ++t) hf[t] = *(volatile LAS v4u*)(hb + t * 64 + kg * 16);
        if (FL4OK(f)) break;
      }
      f32x4 a0 = z, a1 = z;
#pragma unroll
      for (int t = 0; t < 7; ++t) { if (t & 1) a1 = mfma32(Bd[t], hf[t], a1); else a0 = mfma32(Bd[t], hf[t], a0); }
      a0 += a1;
      st_sc1(rs, xst, mkF(a0[0], a0[1], a0[2], a0[3], T));
    }
    if (layer == 0) STAMP(10);
    RESYNC(QUAD_TMAX(ds.z), layer == 0 ? 1 : 6, NOM_G);
    SLIP(layer == 0 ? 2 : 7, layer == 0 ? 125 : 105);
  }
  if (layer == 0 || (g == 0 && (c & 7) == 0)) {
    int xoff = WS_X + layer * 131072 + g * 16384 + (lane >> 3) * 2048 + (32 * kq + xun) * 16;
    int qst = (lane & 0x33) == 0 ? grp + GO_Q1 + kq * 2048 + ((c & 7) * 64 + w * 16 + i16) * 4 : OOB;
    PIN(xoff); PIN(qst);
    WAIT_AT(X.tref, layer == 0 ? 3 : 9);
    LAS unsigned char* xb = L + L_XB + XB_SZ;
    unsigned p;
    v4u r0 = ld_sc1(rs, xoff);
    for (unsigned spins = 0;; ++spins) {
      const v4u d0 = unF(r0, T);
      float t = rows_reduce4(__builtin_bit_cast(f32x4, d0));
      t += dpp<ROR8>(t);
      p = pk(xr + t, 0.f);
      if (layer == 0) *(LAS u16*)(xb + (4 * xun + kg) * 2) = (u16)p;
      const bool ok = (or4(d0) & 0xffu) == 0u;
      RETRY(6, r0 = ld_sc1(rs, xoff));
    }
    if (layer == 0) {
      xr = bflo(p);
      X_SHARE(xb, 8, 14);
      X_FETCH(xb, 8);
      const float part = gemv128(a0, a1, Bq);
      const float p1 = dpp<0x55>(part), p2 = dpp<0xAA>(part), p3 = dpp<0xFF>(part);
      st_sc1(rs, qst, mkF(part, p1, p2, p3, T));
      if ((c & 7) == 0 && w == 0) STATS_PUBLISH(xb, 14, GO_ST2);
      STAMP(11);
      SLIP(3, 100);
    } else {
      if ((lane & 8) == 0) *(GAS u16*)(X.out + 128 * kq + 4 * xun + kg) = (u16)p;
      if (c == 0 && w == 0 && lane == 0) st1_sc1(rs, 0, X.E + 1u);
      STAMP(21);
    }
  }
}

__global__ void __launch_bounds__(512, 2)
fused_decode(const u16* __restrict__ x, const u16* __restrict__ kc, const u16* __restrict__ vc, const u16* __restrict__ wq,
             const u16* __restrict__ wo, const u16* __restrict__ wg, const u16* __restrict__ wu, const u16* __restrict__ wd,
             u16* __restrict__ out, unsigned char* __restrict__ ws) {
  extern __shared__ __attribute__((aligned(16))) unsigned char smem[];
  Ctx X;
  X.vc = vc; X.wo = wo; X.wg = wg; X.wu = wu; X.wd = wd; X.out = out; X.ws = ws;
  X.L = (LAS unsigned char*)smem;
  const int tid = threadIdx.x;
  X.lane = tid & 63; X.wv = __builtin_amdgcn_readfirstlane(tid >> 6); X.w = X.wv & 3; X.kg = X.lane >> 4; X.i16 = X.lane & 15;
  X.b = blockIdx.x; X.g = X.b & 7; X.c = X.b >> 3;
  X.rs = __builtin_amdgcn_make_buffer_rsrc((void*)ws, 0, WS_BYTES, 0x00020000);
  STAMP(0);
  X.t0 = rtc();
  const int lane = X.lane, w = X.w, kg = X.kg, i16 = X.i16, g = X.g, c = X.c, kq = c >> 3;
  const __amdgpu_buffer_rsrc_t rs = X.rs; LAS unsigned char* L = X.L;
  unsigned pfv;
  {
    unsigned long long pc; unsigned len;
    asm volatile("s_getpc_b64 %0\n\tLpf_here:\n\ts_mov_b32 %1, Lpf_end - Lpf_here" : "=s"(pc), "=s"(len));
    unsigned off = (unsigned)(c * 8 + X.wv) * 128u; off = off < len ? off : 0u;
    pfv = *(const GAS unsigned*)(pc + off);
  }
  SCHED();
  asm volatile("" :: "s"(x), "s"(kc), "s"(vc), "s"(wq), "s"(wo), "s"(wg), "s"(wu), "s"(wd), "s"(out), "s"(ws));
  v4u Kf[8], Vf[8], Sw[10], wr[4], xf; v2u xa, xb; unsigned xu, E;
  if (X.wv >= 4) {
    DELAY(12);
    const size_t kvbase = ((size_t)g * SK + (SK - WIN) + 128 * c + 32 * w) * DH;
#pragma unroll
    for (int s = 0; s < 8; ++s) Kf[s] = ldg_nt(kc + kvbase + s * 512 + lane * 8);
#pragma unroll
    for (int j = 0; j < VPRE; ++j) Vf[j] = ldg_nt(vc + kvbase + (16 * (j >> 2) + 4 * kg + (j & 3)) * DH + 8 * i16);
  } else {
#pragma unroll
    for (int n = 0; n < 4; ++n) wr[n] = ldg_nt(wq + (size_t)(128 * kq + 32 * w + 8 * n + (lane >> 3)) * 4096 + 512 * g + 64 * (c & 7) + 8 * (lane & 7));
    xf = *(const GAS v4u*)(x + 8 * lane);
    xa = *(const GAS v2u*)(x + 4 * (32 * kq + 4 * kg + (i16 & 3))); xb = *(const GAS v2u*)(x + 4 * (32 * kq + 16 + 4 * kg + (i16 & 3)));
    xu = *(const GAS u16*)(x + 128 * kq + 4 * (8 * w + (lane & 7)) + kg);
    E = ld1_sc1(rs, 0);
    SCHED();
    {
      const v4u zz = {0u, 0u, 0u, 0u};
      const int slot = ((lane >> 4) & 1) * 4 + (lane & 3), rep = ((lane >> 2) & 3) + 4 * (lane >> 5), grp = WS_GRP + g * GRP_SZ;
      if (w == TW) {
        st_sc1(rs, WS_Y + rep * 32768 + (2 * g + (c & 1)) * 2048 + (c >> 1) * 128 + slot * 16, zz);
        st_sc1(rs, i16 < 8 ? WS_X + i16 * 16384 + g * 2048 + c * 64 + kg * 16 : OOB, zz);
      }
      st_sc1(rs, (c & 7) < 7 ? ((lane & 0x33) == 0 ? grp + GO_G + kq * 2048 + (w >> 1) * 1024 + (8 * (c & 7) + 4 * (w & 1) + (i16 >> 2)) * 16 : OOB)
                             : ((w == 0 && lane == 0) ? grp + GO_ST + kq * 16 : OOB), zz);
    }
  }
  SCHED();
  int wv2 = X.wv; asm volatile("" : "+s"(wv2));
  if (wv2 >= 4) run_A(X, Kf, Vf, Sw);
  int wv3 = X.wv; asm volatile("" : "+s"(wv3));
  if (wv3 < 4) {
#pragma unroll
    for (int n = 0; n < 4; ++n) *(LAS v4u*)(L + L_IMG + w * 4096 + n * 1024 + lane * 16) = wr[n];
    const unsigned hsh = (E + 1u) * 0x9E3779B1u;
    const unsigned T = __builtin_amdgcn_readfirstlane(((hsh & 0xbfffbf00u) | (((hsh >> 8) & 0xbfu) ^ 0xa5u)));
    X.E = E; X.T = T;
    LPOST(0);
    float xr = bflo(xu);
    v2u Bq[8], Bg[8]; v4u Aw[12], Bd[7];
    {
      float xv[8] = {bflo(xf.x), bfhi(xf.x), bflo(xf.y), bfhi(xf.y), bflo(xf.z), bfhi(xf.z), bflo(xf.w), bfhi(xf.w)};
      float s = 0.f;
#pragma unroll
      for (int k = 0; k < 8; ++k) s += xv[k];
      const float mean = xrow_sum(row_sum(s)) * (1.f / DM);
      float v = 0.f;
#pragma unroll
      for (int k = 0; k < 8; ++k) { const float d = xv[k] - mean; v = fmaf(d, d, v); }
      const float var = xrow_sum(row_sum(v)) * (1.f / DM);
      const float mb = rbf(mean), isd = 1.0f / rbf(sqrtf(rbf(rbf(var) + 1e-5f)));
      const float xi[8] = {bflo(xa.x), bfhi(xa.x), bflo(xa.y), bfhi(xa.y), bflo(xb.x), bfhi(xb.x), bflo(xb.y), bfhi(xb.y)};
      float xn[8];
#pragma unroll
      for (int k = 0; k < 8; ++k) xn[k] = rbf(xi[k] - mb) * isd;
      const v2u a0 = {pk(xn[0], xn[1]), pk(xn[2], xn[3])}, a1 = {pk(xn[4], xn[5]), pk(xn[6], xn[7])};
      LWAIT4(0);
#pragma unroll
      for (int u = 0; u < 8; ++u) Bq[u] = trread(L + L_IMG + (64 * (u >> 2) + 16 * kg + 4 * (u & 3) + (i16 >> 2)) * 128 + w * 32 + (i16 & 3) * 8);
      const int qo = (lane & 0x33) == 0 ? WS_Q0 + g * 8192 + kq * 2048 + ((c & 7) * 64 + w * 16 + i16) * 4 : OOB - 65536;
      {
        const float part = gemv128(a0, a1, Bq);
        const float p1 = dpp<0x55>(part), p2 = dpp<0xAA>(part), p3 = dpp<0xFF>(part);
        st_sc1(rs, qo, mkF(part, p1, p2, p3, T));
      }
      STAMP(1);
      {
        const v2u one = {ONES, ONES};
        const float part = gemv128(one, one, Bq);
        const float p1 = dpp<0x55>(part), p2 = dpp<0xAA>(part), p3 = dpp<0xFF>(part);
        st_sc1(rs, qo + 65536, mkF(part, p1, p2, p3, T));
      }
    }
    f32x4 csq = {0.f, 0.f, 0.f, 0.f}; float csg[2] = {0.f, 0.f};
    run_B<0>(X, Bq, Aw, Bg, Bd, xr, csq, csg);
    run_B<1>(X, Bq, Aw, Bg, Bd, xr, csq, csg);
  }
  asm volatile("" :: "v"(pfv));
  STAMP_FLUSH();
  asm volatile("Lpf_end:");
}

extern "C" void kernel_launch(void* const* d_in, const int* in_sizes, int n_in, void* d_out, int out_size, void* d_ws, size_t ws_size,
                              hipStream_t stream) {
  (void)in_sizes; (void)n_in; (void)out_size; (void)ws_size;
  (void)hipFuncSetAttribute((const void*)fused_decode, hipFuncAttributeMaxDynamicSharedMemorySize, LDS_BYTES);
  hipLaunchKernelGGL(fused_decode, dim3(256), dim3(512), LDS_BYTES, stream, (const u16*)d_in[0], (const u16*)d_in[1], (const u16*)d_in[2],
                     (const u16*)d_in[3], (const u16*)d_in[4], (const u16*)d_in[6], (const u16*)d_in[7], (const u16*)d_in[8], (u16*)d_out,
                     (unsigned char*)d_ws);
}
```

```cpp
#define DIAG 6
#include <hip/hip_runtime.h>
#include <stdint.h>

typedef unsigned short u16;
typedef unsigned v4u __attribute__((ext_vector_type(4)));
typedef unsigned v2u __attribute__((ext_vector_type(2)));
typedef float f32x4 __attribute__((ext_vector_type(4)));
typedef float f32x2 __attribute__((ext_vector_type(2)));
typedef short s16x4 __attribute__((ext_vector_type(4)));
typedef __bf16 bf16x8 __attribute__((ext_vector_type(8)));
typedef __bf16 bf16x2 __attribute__((ext_vector_type(2)));
#define LAS __attribute__((address_space(3)))
#define GAS __attribute__((address_space(1)))

constexpr int SK = 32768, WIN = 4096, DH = 128, DM = 512, FF = 1792;
constexpr int WS_FO = 4096;
constexpr int WS_FY = WS_FO + 32768;
constexpr int WS_X = WS_FY + 8192;
constexpr int WS_Y = WS_X + 262144;
constexpr int WS_Q0 = WS_Y + 524288;
constexpr int WS_GC = WS_Q0 + 131072;
constexpr int WS_GRP = WS_GC + 65536, GRP_SZ = 143360, O_L = 61440;
constexpr int GO_ML = 0;
constexpr int GO_O = 2048;
constexpr int GO_G = 51200;
constexpr int GO_ST = 59392;
constexpr int GO_ST2 = O_L + 61440 + 8192;
constexpr int GO_Q1 = O_L + 61440;
static_assert(GO_ST2 + 128 <= GRP_SZ && 128 * 480 == O_L, "group block");
constexpr int WS_BYTES = WS_GRP + 8 * GRP_SZ;
constexpr int OOB = 0x7fffff00;
constexpr int WS_STAMP = 8 << 20;
constexpr int L_IMG = 0;
constexpr int L_IGU = 16384;
constexpr int L_IDN = 32768;
constexpr int L_IWO = 40960;
constexpr int L_STG = 57344, STG_W = 8704;
constexpr int L_ATT = L_STG + 4 * STG_W;
constexpr int L_ML = L_ATT + 8192;
constexpr int L_QB = L_ML + 128, QSTR = 320;
constexpr int L_XB = L_QB + 4 * QSTR;
constexpr int XB_SZ = 384;
constexpr int L_HB = L_XB + 3 * XB_SZ;
constexpr int L_FL = L_HB + 1024;
constexpr int L_RED = L_FL + 256, RED_ROW = 144, RED_SZ = 4 * RED_ROW;
constexpr int LDS_BYTES = 112 * 1024;
static_assert(L_RED + 2 * RED_SZ <= LDS_BYTES, "lds");
constexpr int TW = 3;
constexpr unsigned SPIN_CAP = 1u << 17;

constexpr int DL[16] = {  105,   290,   420,   555,   675,   825,   970,   1090,
                          3,   1220,   1,   340,   20,
                          15,   4,   40};

constexpr int NOM_G = 85, NOM_Q = 92, NOM_O = 120;
#define SCHED() __builtin_amdgcn_sched_barrier(0)
#define STAMP(i) do {} while (0)
#define PASS(i) do {} while (0)
#define STAMP_FLUSH() do {} while (0)
#define DELAY(i) do { if (DL[i] > 0) __builtin_amdgcn_s_sleep(DL[i]); } while (0)
#define DLV(i) DL[i]
__device__ __forceinline__ unsigned rtc() { return (unsigned)__builtin_amdgcn_s_memrealtime(); }
#define WAIT_AT(ref, i) do { SCHED(); unsigned tg_ = (ref) + (unsigned)DLV(i); const unsigned lo_ = rtc() + (unsigned)DLV(13); if ((int)(lo_ - tg_) > 0) tg_ = lo_; \
    for (int k_ = 0; k_ < 4096 && (int)(rtc() - tg_) < 0; ++k_) {} SCHED(); } while (0)
#define PW_A 0
#define VPRE 0
#define DN_LATE 1
#define PW_B 1
#define UNLIKELY(x) __builtin_expect(!!(x), 0)
#define LIKELY(x) __builtin_expect(!!(x), 1)
#define PIN(v) asm volatile("" : "+v"(v))

__device__ __forceinline__ float bflo(unsigned u) { return __uint_as_float(u << 16); }
__device__ __forceinline__ float bfhi(unsigned u) { return __uint_as_float(u & 0xffff0000u); }
__device__ __forceinline__ unsigned pk(float lo, float hi) { bf16x2 p = {(__bf16)lo, (__bf16)hi}; return __builtin_bit_cast(unsigned, p); }
__device__ __forceinline__ float rbf(float f) { return bflo((unsigned)__builtin_bit_cast(u16, (__bf16)f)); }
__device__ __forceinline__ float vmax(float a, float b) { return __builtin_elementwise_maximum(a, b); }
template <int CTRL> __device__ __forceinline__ float dpp(float x) {
  return __builtin_bit_cast(float, __builtin_amdgcn_mov_dpp(__builtin_bit_cast(int, x), CTRL, 0xf, 0xf, true));
}
template <int CTRL> __device__ __forceinline__ unsigned dppu(unsigned x) { return (unsigned)__builtin_amdgcn_mov_dpp((int)x, CTRL, 0xf, 0xf, true); }
constexpr int XOR1 = 0xB1, XOR2 = 0x4E, HMIR = 0x141, MIR = 0x140, ROR4 = 0x124, ROR8 = 0x128;
__device__ __forceinline__ float row_sum(float v) { v += dpp<XOR1>(v); v += dpp<XOR2>(v); v += dpp<HMIR>(v); v += dpp<MIR>(v); return v; }
__device__ __forceinline__ float row_max(float v) { v = vmax(v, dpp<XOR1>(v)); v = vmax(v, dpp<XOR2>(v)); v = vmax(v, dpp<HMIR>(v)); v = vmax(v, dpp<MIR>(v)); return v; }
__device__ __forceinline__ float xrow_sum(float x) {
  auto s = __builtin_amdgcn_permlane16_swap(__float_as_uint(x), __float_as_uint(x), false, false);
  x = __uint_as_float(s[0]) + __uint_as_float(s[1]);
  auto t = __builtin_amdgcn_permlane32_swap(__float_as_uint(x), __float_as_uint(x), false, false);
  return __uint_as_float(t[0]) + __uint_as_float(t[1]);
}
__device__ __forceinline__ float xrow_max(float x) {
  auto s = __builtin_amdgcn_permlane16_swap(__float_as_uint(x), __float_as_uint(x), false, false);
  x = vmax(__uint_as_float(s[0]), __uint_as_float(s[1]));
  auto t = __builtin_amdgcn_permlane32_swap(__float_as_uint(x), __float_as_uint(x), false, false);
  return vmax(__uint_as_float(t[0]), __uint_as_float(t[1]));
}
__device__ __forceinline__ float rows_reduce4(f32x4 v) {
  auto a = __builtin_amdgcn_permlane16_swap(__float_as_uint(v[0]), __float_as_uint(v[1]), false, false);
  auto b = __builtin_amdgcn_permlane16_swap(__float_as_uint(v[2]), __float_as_uint(v[3]), false, false);
  const float t01 = __uint_as_float(a[0]) + __uint_as_float(a[1]), t23 = __uint_as_float(b[0]) + __uint_as_float(b[1]);
  auto c = __builtin_amdgcn_permlane32_swap(__float_as_uint(t01), __float_as_uint(t23), false, false);
  return __uint_as_float(c[0]) + __uint_as_float(c[1]);
}
__device__ __forceinline__ f32x4 mfma32(v4u a, v4u b, f32x4 c) {
  return __builtin_amdgcn_mfma_f32_16x16x32_bf16(__builtin_bit_cast(bf16x8, a), __builtin_bit_cast(bf16x8, b), c, 0, 0, 0);
}
__device__ __forceinline__ f32x4 mfma16(v2u a, v2u b, f32x4 c) {
  return __builtin_amdgcn_mfma_f32_16x16x16bf16_1k(__builtin_bit_cast(s16x4, a), __builtin_bit_cast(s16x4, b), c, 0, 0, 0);
}
__device__ __forceinline__ float dot2(unsigned a, unsigned b, float c) {
  return __builtin_amdgcn_fdot2_f32_bf16(__builtin_bit_cast(bf16x2, a), __builtin_bit_cast(bf16x2, b), c, false);
}
__device__ __forceinline__ v2u trread(LAS unsigned char* p) { return __builtin_bit_cast(v2u, __builtin_amdgcn_ds_read_tr16_b64_v4i16((LAS s16x4*)p)); }
__device__ __forceinline__ v4u ldg_nt(const u16* p) { return __builtin_nontemporal_load((const GAS v4u*)p); }
__device__ __forceinline__ v4u ld_sc1(__amdgpu_buffer_rsrc_t rs, int off) { return __builtin_amdgcn_raw_buffer_load_b128(rs, off, 0, 16); }
__device__ __forceinline__ unsigned ld1_sc1(__amdgpu_buffer_rsrc_t rs, int off) { return __builtin_amdgcn_raw_buffer_load_b32(rs, off, 0, 16); }
__device__ __forceinline__ v2u ld2_sc1(__amdgpu_buffer_rsrc_t rs, int off) { return __builtin_amdgcn_raw_buffer_load_b64(rs, off, 0, 16); }
__device__ __forceinline__ void st2_sc1(__amdgpu_buffer_rsrc_t rs, int off, v2u v) { __builtin_amdgcn_raw_buffer_store_b64(v, rs, off, 0, 16); }
__device__ __forceinline__ void st_sc1(__amdgpu_buffer_rsrc_t rs, int off, v4u v) { __builtin_amdgcn_raw_buffer_store_b128(v, rs, off, 0, 16); }
__device__ __forceinline__ void st1_sc1(__amdgpu_buffer_rsrc_t rs, int off, unsigned v) { __builtin_amdgcn_raw_buffer_store_b32(v, rs, off, 0, 16); }
#define PERM(hi, lo, sel) __builtin_amdgcn_perm((hi), (lo), (sel))
__device__ __forceinline__ bool okH(v4u d, unsigned T) { return (PERM(d.y, d.x, 0x05040100u) == T) & (PERM(d.w, d.z, 0x05040100u) == T); }
__device__ __forceinline__ v2u valH(v4u d) { return v2u{PERM(d.y, d.x, 0x07060302u), PERM(d.w, d.z, 0x07060302u)}; }
__device__ __forceinline__ v4u mkH(unsigned p0, unsigned p1, unsigned T) {
  return v4u{PERM(p0, T, 0x05040100u), PERM(p0, T, 0x07060302u), PERM(p1, T, 0x05040100u), PERM(p1, T, 0x07060302u)};
}
__device__ __forceinline__ v4u unF(v4u d, unsigned T) { return v4u{d.x ^ (T & 0xffu), d.y ^ ((T >> 8) & 0xffu), d.z ^ ((T >> 16) & 0xffu), d.w ^ (T >> 24)}; }
__device__ __forceinline__ unsigned or4(v4u d) { return (d.x | d.y) | (d.z | d.w); }
__device__ __forceinline__ v4u mkF(float a, float b, float c, float d, unsigned T) {
  return v4u{PERM(__float_as_uint(a), T, 0x07060500u), PERM(__float_as_uint(b), T, 0x07060501u),
             PERM(__float_as_uint(c), T, 0x07060502u), PERM(__float_as_uint(d), T, 0x07060503u)};
}
#define LGKM0() asm volatile("s_waitcnt lgkmcnt(0)" ::: "memory")
#define BARRIER() do { LGKM0(); __builtin_amdgcn_s_barrier(); asm volatile("" ::: "memory"); } while (0)

__device__ __forceinline__ float gemv128(v2u a0, v2u a1, const v2u (&B)[8]) {
  const f32x4 z = {0.f, 0.f, 0.f, 0.f};
  f32x4 c0 = mfma16(a0, B[0], z), c1 = mfma16(a0, B[1], z), c2 = mfma16(a0, B[2], z), c3 = mfma16(a0, B[3], z);
  c0 = mfma16(a1, B[4], c0); c1 = mfma16(a1, B[5], c1); c2 = mfma16(a1, B[6], c2); c3 = mfma16(a1, B[7], c3);
  return (c0[0] + c1[1]) + (c2[2] + c3[3]);
}
struct Ctx {
  const u16 *vc, *wo, *wg, *wu, *wd; u16* out; unsigned char* ws;
  __amdgpu_buffer_rsrc_t rs; LAS unsigned char* L;
  int lane, w, wv, kg, i16, b, g, c; unsigned E, T, t0, tref, dv;
};

#define LPOST(set) do { asm volatile("" ::: "memory"); *(volatile LAS unsigned*)(L + L_FL + (set) * 16 + w * 4) = T; asm volatile("" ::: "memory"); } while (0)
#define LWAIT4(set) do { for (unsigned spins_ = 0; spins_ < (1u << 24); ++spins_) { const v4u f_ = *(volatile LAS v4u*)(L + L_FL + (set) * 16); \
      if ((f_.x == T) & (f_.y == T) & (f_.z == T) & (f_.w == T)) break; } asm volatile("" ::: "memory"); } while (0)
#define LWAIT1(set, who) do { for (unsigned spins_ = 0; spins_ < (1u << 24); ++spins_) { \
      if (*(volatile LAS unsigned*)(L + L_FL + (set) * 16 + (who) * 4) == T) break; __builtin_amdgcn_s_sleep(1); } asm volatile("" ::: "memory"); } while (0)
#define ONES 0x3f803f80u
#define FL4(set) (*(volatile LAS v4u*)(L + L_FL + (set) * 16))
#define FL4OK(f) (((f).x == T) & ((f).y == T) & ((f).z == T) & ((f).w == T))

__device__ __forceinline__ v4u load_sw(const Ctx& X, int n) {
  const int cb = (X.c & 7) < 7 ? (X.c & 7) : 6;
  if (n < 4) return ldg_nt(X.wo + (size_t)(512 * X.g + 256 * (X.c & 1) + 64 * X.w + 16 * n + (X.lane >> 2)) * DM + 32 * (X.c >> 1) + 8 * (X.lane & 3));
  if (n < 8) return ldg_nt((n >= 6 ? X.wu : X.wg) + (size_t)(128 * (X.c >> 3) + 32 * X.w + 16 * (n & 1) + (X.lane >> 2)) * FF + 224 * X.g + 32 * cb + 8 * (X.lane & 3));
  const int blk = 2 * X.w + (n & 1) < 7 ? 2 * X.w + (n & 1) : 6;
  return ldg_nt(X.wd + (size_t)(224 * X.g + 32 * blk + (X.lane >> 1)) * DM + 16 * X.c + 8 * (X.lane & 1));
}
__device__ __forceinline__ void run_A(Ctx& X, v4u (&Kf)[8], v4u (&Vf)[8], v4u (&Sw)[10]) {
  const int lane = X.lane, w = X.w, kg = X.kg, i16 = X.i16, g = X.g, c = X.c;
  const __amdgpu_buffer_rsrc_t rs = X.rs; LAS unsigned char* L = X.L; LAS unsigned char* stg = L + L_STG + w * STG_W;
  const f32x4 z = {0.f, 0.f, 0.f, 0.f};
  STAMP(12);
  if (DIAG == 6 && w == 0) { asm volatile("" :: "v"(Kf[0])); *(volatile LAS unsigned*)(L + L_FL + 11 * 16 + 256 + 2048) = rtc(); }
  const int u = (lane * 43) >> 7;
  const bool head3 = lane == 3 * u;
  int ooff = head3 ? WS_GRP + g * GRP_SZ + GO_O + w * 12288 + u * 512 + c * 16 : (lane == 1 ? WS_GRP + g * GRP_SZ + GO_ML + w * 512 + c * 16 : OOB);
  const int ostep = (head3 || lane == 1) ? O_L : 0;
  const u16* vb = X.vc + ((size_t)g * SK + (SK - WIN) + 128 * c + 32 * w) * DH;
  {
#pragma unroll
    for (int s = 0; s < 8; ++s) {
      *(LAS v4u*)(stg + (4 * s + kg) * 272 + i16 * 16) = Kf[s];
      SCHED();
      if (s + VPRE < 8) { const int j = s + VPRE; Vf[j] = ldg_nt(vb + (16 * (j >> 2) + 4 * kg + (j & 3)) * DH + 8 * i16); }
      SCHED();
    }
    LGKM0();
#pragma unroll
    for (int s = 0; s < 8; ++s) Kf[s] = *(LAS v4u*)(stg + (16 * (s >> 2) + i16) * 272 + (s & 3) * 64 + kg * 16);
  }
  {
    v4u t[8];
#pragma unroll
    for (int jj = 0; jj < 4; ++jj) {
#pragma unroll
      for (int n = 0; n < 8; ++n) t[n][jj] = PERM(Vf[2 * jj + 1][n >> 1], Vf[2 * jj][n >> 1], (n & 1) ? 0x07060302u : 0x05040100u);
      asm volatile("" : "+v"(t[0][jj]), "+v"(t[7][jj]));
      SCHED();
      Sw[jj] = load_sw(X, jj);
      SCHED();
    }
#pragma unroll
    for (int n = 0; n < 8; ++n) Vf[n] = t[n];
    asm volatile("" :: "v"(Vf[0]), "v"(Vf[1]), "v"(Vf[2]), "v"(Vf[3]), "v"(Vf[4]), "v"(Vf[5]), "v"(Vf[6]), "v"(Vf[7]));
  }
  STAMP(13);
  if (DIAG == 5 && w == 0) *(volatile LAS unsigned*)(L + L_FL + 11 * 16 + 256 + 2048) = rtc();
#pragma unroll
  for (int n = 0; n < 4; ++n) { *(LAS v4u*)(L + L_IWO + w * 4096 + n * 1024 + lane * 16) = Sw[n]; SCHED(); Sw[4 + n] = load_sw(X, 4 + n); SCHED(); }
#pragma unroll
  for (int n = 0; n < 4; ++n) {
    *(LAS v4u*)(L + L_IGU + (n >> 1) * 8192 + w * 2048 + (n & 1) * 1024 + lane * 16) = Sw[4 + n]; SCHED();
    if (n & 1) { Sw[8 + (n >> 1)] = load_sw(X, 8 + (n >> 1)); SCHED(); }
  }
  if (DN_LATE == 0) {
#pragma unroll
    for (int n = 0; n < 2; ++n) *(LAS v4u*)(L + L_IDN + (2 * w + n) * 1024 + lane * 16) = Sw[8 + n];
  }
  STAMP(9);
#pragma unroll
  for (int layer = 0; layer < 2; ++layer) {
    BARRIER();
    const unsigned T = __builtin_amdgcn_readfirstlane(*(volatile LAS unsigned*)(L + L_FL));
    {
      v4u qf[4];
#pragma unroll
      for (int s = 0; s < 4; ++s) qf[s] = *(LAS v4u*)(L + L_QB + (i16 >> 2) * QSTR + s * 64 + kg * 16);
      f32x4 sa = z, sb = z;
#pragma unroll
      for (int s = 0; s < 4; ++s) { sa = mfma32(Kf[s], qf[s], sa); sb = mfma32(Kf[4 + s], qf[s], sb); }
      float sv[8];
      if (layer == 0) {
#pragma unroll
        for (int k = 0; k < 8; k += 2) {
          const unsigned p = k < 4 ? pk(sa[k & 3], sa[(k & 3) + 1]) : pk(sb[k & 3], sb[(k & 3) + 1]);
          const unsigned r = pk(bflo(p) * 0.08838834764831845f, bfhi(p) * 0.08838834764831845f);
          sv[k] = bflo(r); sv[k + 1] = bfhi(r);
        }
      } else {
#pragma unroll
        for (int k = 0; k < 4; ++k) { sv[k] = sa[k]; sv[4 + k] = sb[k]; }
      }
      const float CE = layer == 0 ? 1.4426950408889634f : 1.4426950408889634f * 0.08838834764831845f;
      const float m = xrow_max(vmax(vmax(vmax(sv[0], sv[1]), vmax(sv[2], sv[3])), vmax(vmax(sv[4], sv[5]), vmax(sv[6], sv[7])))) * CE;
#pragma unroll
      for (int k = 0; k < 8; ++k) sv[k] = __builtin_amdgcn_exp2f(fmaf(sv[k], CE, -m));
      const v4u pf = {pk(sv[0], sv[1]), pk(sv[2], sv[3]), pk(sv[4], sv[5]), pk(sv[6], sv[7])};
      f32x4 o[8];
#pragma unroll
      for (int n = 0; n < 8; ++n) o[n] = mfma32(pf, Vf[n], z);
      const f32x4 lv = mfma32(pf, v4u{ONES, ONES, ONES, ONES}, z);
      LAS unsigned char* op = L + L_ATT + w * 2048 + kg * 512 + i16 * 32;
      *(LAS f32x4*)op = f32x4{o[0][0], o[1][1], o[2][2], o[3][3]};
      *(LAS f32x4*)(op + 16) = f32x4{o[4][0], o[5][1], o[6][2], o[7][3]};
      if (i16 == 4 * kg) *(LAS f32x2*)(L + L_ML + kg * 32 + w * 8) = f32x2{m, lv[0]};
    }
    LPOST(1 + layer);
    const unsigned tm = rtc();
    {
      f32x4 ml0, ml1; f32x2 ov[4];
      for (unsigned sp = 0; sp < (1u << 24); ++sp) {
        const v4u f = FL4(1 + layer);
        ml0 = *(volatile LAS f32x4*)(L + L_ML + w * 32); ml1 = *(volatile LAS f32x4*)(L + L_ML + w * 32 + 16);
#pragma unroll
        for (int k = 0; k < 4; ++k) ov[k] = *(volatile LAS f32x2*)(L + L_ATT + k * 2048 + w * 512 + lane * 8);
        if (FL4OK(f)) break;
      }
      const float M = vmax(vmax(ml0[0], ml0[2]), vmax(ml1[0], ml1[2]));
      const float e0 = __builtin_amdgcn_exp2f(ml0[0] - M), e1 = __builtin_amdgcn_exp2f(ml0[2] - M), e2 = __builtin_amdgcn_exp2f(ml1[0] - M), e3 = __builtin_amdgcn_exp2f(ml1[2] - M);
      const f32x2 a = (ov[0] * e0 + ov[1] * e1) + (ov[2] * e2 + ov[3] * e3);
      const float ls = fmaf(e0, ml0[1], e1 * ml0[3]) + fmaf(e2, ml1[1], e3 * ml1[3]);
      unsigned dv = pk(a[0], a[1]);
      unsigned d1 = dppu<0x130>(dv);
      unsigned d2 = dppu<0x130>(d1);
      dv = head3 ? dv : __float_as_uint(M); d1 = head3 ? d1 : __float_as_uint(ls); d2 = head3 ? d2 : tm;
      st_sc1(rs, ooff, v4u{dv, d1, d2, T});
      ooff += ostep; asm volatile("" : "+v"(ooff));
    }
    if (layer == 0) { STAMP(3);
      if (DN_LATE) {
#pragma unroll
        for (int n = 0; n < 2; ++n) *(LAS v4u*)(L + L_IDN + (2 * w + n) * 1024 + lane * 16) = Sw[8 + n];
      }
      LPOST(15);
    } else STAMP(15);
  }
}

#define RETRY(pi, reload) if (LIKELY(__all(ok)) || spins > SPIN_CAP) break; asm volatile("" ::: "memory"); DELAY(8); PASS(pi); if (!ok) { reload; }
#define SLIP(i, lat) do { const int late_ = (int)(rtc() - (X.tref + (unsigned)DLV(i) + (unsigned)(lat))); if (late_ > 0) X.tref += (unsigned)(late_ + DLV(15)); } while (0)
#define RESYNC(tmax, i, nom) do { const int late_ = (int)((tmax) - (X.tref + (unsigned)DLV(i) + (unsigned)(nom))); if (late_ > 0) X.tref += (unsigned)late_; } while (0)
#define QUAD_TMAX(tv) ({ int dt_ = (int)((tv) - X.tref); dt_ = max(dt_, (int)dppu<XOR1>((unsigned)dt_)); dt_ = max(dt_, (int)dppu<XOR2>((unsigned)dt_)); X.tref + (unsigned)__builtin_amdgcn_readfirstlane(dt_); })
#define LN_STATS(ds) float s1 = __uint_as_float((ds).x), s2 = __uint_as_float((ds).y); \
      s1 += dpp<XOR1>(s1); s2 += dpp<XOR1>(s2); s1 += dpp<XOR2>(s1); s2 += dpp<XOR2>(s2); \
      const float mean = s1 * (1.f / DM), rstd = __builtin_amdgcn_rsqf(fmaxf(fmaf(-mean, mean, s2 * (1.f / DM)), 0.f) + 1e-5f)

template <int layer>
__device__ __forceinline__ void run_B(Ctx& X, const v2u (&Bq)[8], v4u (&Aw)[12], v2u (&Bg)[8], v4u (&Bd)[7], float& xr, f32x4& csq, float (&csg)[2]) {
  const int lane = X.lane, w = X.w, kg = X.kg, i16 = X.i16, g = X.g, c = X.c, kq = c >> 3;
  const unsigned T = X.T; const __amdgpu_buffer_rsrc_t rs = X.rs; LAS unsigned char* L = X.L;
  LAS unsigned char* red = L + L_RED + layer * RED_SZ;
  const f32x4 z = {0.f, 0.f, 0.f, 0.f};
  const int grp = WS_GRP + g * GRP_SZ;
  v2u fy0; v4u c0, c1; unsigned tq;
  const int qun = kg * 8 + (lane & 7), qidx = ((lane >> 3) & 1) * 2048 + (32 * w + qun) * 16;
  const int xun = 8 * w + (lane & 7);
  {
    int qoff = (layer == 0 ? WS_Q0 + g * 8192 : grp + GO_Q1) + qidx, soff = grp + GO_ST2 + (lane & 3) * 16;
    PIN(qoff); PIN(soff);
    if (layer == 0) WAIT_AT(X.t0, 11); else WAIT_AT(X.tref, 4);
    v4u ds = v4u{0u, 0u, 0u, T}, r0, r1;
#define QLOADS() do { if (layer == 1) ds = ld_sc1(rs, soff); r0 = ld_sc1(rs, qoff); r1 = ld_sc1(rs, qoff + 4096); } while (0)
    QLOADS();
    SCHED();
    for (unsigned spins = 0;; ++spins) {
      LN_STATS(ds);
      const v4u d0 = unF(r0, T), d1 = unF(r1, T);
      float qv[4];
#pragma unroll
      for (int k = 0; k < 4; ++k) {
        float t = __uint_as_float(d0[k]) + __uint_as_float(d1[k]); t += dpp<ROR8>(t);
        qv[k] = layer == 0 ? t : rstd * fmaf(-mean, csq[k], t);
      }
      *(LAS v2u*)(L + L_QB + w * QSTR + qun * 8) = v2u{pk(qv[0], qv[1]), pk(qv[2], qv[3])};
      const bool ok = (((or4(d0) | or4(d1)) & 0xffu) == 0u) & (ds.w == T);
      RETRY(layer ? 7 : 0, QLOADS());
    }
    tq = ds.z;
    STAMP(2 + 12 * layer);
    if (DIAG == 3 && layer == 0) X.dv = rtc() - X.t0;
  }
  BARRIER();
  if (layer == 1) { RESYNC(QUAD_TMAX(tq), 3, NOM_Q); SLIP(4, 72); }
  if (layer == 0) {
    tq = rtc();
    if (w == TW && lane == 0) st2_sc1(rs, WS_FO + (g * 32 + c) * 128, v2u{T, tq});
    fy0 = ld2_sc1(rs, w == TW ? WS_FO + (g * 32 + (lane & 31)) * 128 : OOB);
    c0 = ld_sc1(rs, WS_Q0 + 65536 + g * 8192 + qidx); c1 = ld_sc1(rs, WS_Q0 + 65536 + g * 8192 + qidx + 4096);
    SCHED();
  }
#define WO_PREP() do { const int hh = w >> 1; \
_Pragma("unroll") \
    for (int t = 0; t < 6; ++t) { \
      const int un = 4 * t + kg, r0 = 6 * un + (i16 >> 2); \
      const int ra = r0 > 127 ? 127 : r0, rb = r0 + 4 > 127 ? 127 : r0 + 4; \
_Pragma("unroll") \
      for (int ct = 0; ct < 2; ++ct) { \
        const v2u lo = trread(L + L_IWO + (hh * 128 + ra) * 64 + ct * 32 + (i16 & 3) * 8), hi = trread(L + L_IWO + (hh * 128 + rb) * 64 + ct * 32 + (i16 & 3) * 8); \
        Aw[t * 2 + ct] = v4u{(t == 5 && un > 21) ? 0u : lo.x, (t == 5 && un > 20) ? 0u : lo.y, (t == 5 && un > 20) ? 0u : hi.x, 0u}; \
      } \
    } \
  } while (0)
  if (layer == 0) {
    WO_PREP();
#pragma unroll
    for (int u = 0; u < 8; ++u) Bg[u] = trread(L + L_IGU + (w >> 1) * 8192 + (64 * (u >> 2) + 16 * kg + 4 * (u & 3) + (i16 >> 2)) * 64 + (w & 1) * 32 + (i16 & 3) * 8);
    const v2u one = {ONES, ONES};
    const float cs = gemv128(one, one, Bg);
    const float p1 = dpp<0x55>(cs), p2 = dpp<0xAA>(cs), p3 = dpp<0xFF>(cs);
    st_sc1(rs, ((lane & 0x33) == 0 && (c & 7) < 7) ? WS_GC + g * 8192 + kq * 2048 + (w >> 1) * 1024 + (8 * (c & 7) + 4 * (w & 1) + (i16 >> 2)) * 16 : OOB, mkF(cs, p1, p2, p3, T));

  }
  {
    const int hh = w >> 1, st = w & 1, h = 2 * (c & 1) + hh;
    int obase = grp + GO_O + layer * O_L + h * 12288 + (16 * st + i16) * 16 + kg * 512;
    int o5 = obase + 5 * 2048 - (kg & 2) * 512;
    const int rot = (4 * kg + i16) & 15;
    int mlA = grp + GO_ML + layer * O_L + h * 512 + (16 * st + rot) * 16, mlB = mlA ^ 256;
    const int slot = ((lane >> 4) & 1) * 4 + (lane & 3), rep = ((lane >> 2) & 3) + 4 * (lane >> 5);
    int yst = WS_Y + layer * 262144 + rep * 32768 + (2 * g + (c & 1)) * 2048 + (c >> 1) * 128 + slot * 16;
    PIN(obase); PIN(o5); PIN(mlA); PIN(mlB); PIN(yst);
    if (layer == 0) {
      unsigned tg;
      if (w == TW) {
        for (unsigned spins = 0;; ++spins) {
          if ((lane & 31) == c) fy0 = v2u{T, tq};
          if (__all(fy0.x == T) || spins > SPIN_CAP) break;
          asm volatile("" ::: "memory"); DELAY(10); PASS(1);
          fy0 = ld2_sc1(rs, WS_FO + (g * 32 + (lane & 31)) * 128);
        }
        int dt = (int)(fy0.y - X.t0); dt = max(dt, (int)dppu<XOR1>((unsigned)dt)); dt = max(dt, (int)dppu<XOR2>((unsigned)dt)); dt = max(dt, (int)dppu<HMIR>((unsigned)dt));
        dt = max(dt, (int)dppu<MIR>((unsigned)dt)); dt = max(__builtin_amdgcn_readlane(dt, 0), __builtin_amdgcn_readlane(dt, 16)); tg = X.t0 + (unsigned)dt;
        *(volatile LAS unsigned*)(L + L_FL + 3 * 16) = tg; LPOST(3);
      } else { LWAIT1(3, TW); tg = __builtin_amdgcn_readfirstlane(*(volatile LAS unsigned*)(L + L_FL + 3 * 16)); }
      if (c == 0 && w == TW && lane < 8) st2_sc1(rs, WS_FY + (lane * 8 + g) * 128, v2u{T, tg});
      for (unsigned spins = 0;; ++spins) {
        c0 = unF(c0, T); c1 = unF(c1, T);
        if (__all(((or4(c0) | or4(c1)) & 0xffu) == 0u) || spins > SPIN_CAP) break;
        asm volatile("" ::: "memory"); DELAY(8); c0 = ld_sc1(rs, WS_Q0 + 65536 + g * 8192 + qidx); c1 = ld_sc1(rs, WS_Q0 + 65536 + g * 8192 + qidx + 4096);
      }
#pragma unroll
      for (int k = 0; k < 4; ++k) { float t = __uint_as_float(c0[k]) + __uint_as_float(c1[k]); t += dpp<ROR8>(t); csq[k] = t; }
      { unsigned t_ = tg + (unsigned)DLV(0); for (int k_ = 0; k_ < 4096 && (int)(rtc() - t_) < 0; ++k_) {} }
      X.tref = tg;
      if (DIAG == 4) X.dv = tg - X.t0;
    } else WAIT_AT(X.tref, 5);
    if (layer == 0) fy0 = ld2_sc1(rs, WS_FY + (g * 8 + (lane & 7)) * 128);
    v4u d[8];
#define OLOADS() do { d[6] = ld_sc1(rs, mlA); d[7] = ld_sc1(rs, mlB); _Pragma("unroll") for (int t = 0; t < 5; ++t) d[t] = ld_sc1(rs, obase + t * 2048); d[5] = ld_sc1(rs, o5); } while (0)
    OLOADS();
    SCHED();
    for (unsigned spins = 0;; ++spins) {
      const float mA = __uint_as_float(d[6].x), lA = __uint_as_float(d[6].y), mB = __uint_as_float(d[7].x), lB = __uint_as_float(d[7].y);
      const float M = row_max(vmax(mA, mB));
      const float eA = __builtin_amdgcn_exp2f(mA - M), eB = __builtin_amdgcn_exp2f(mB - M);
      const float Ls = row_sum(fmaf(eA, lA, eB * lB));
      const float wgt = eA * __builtin_amdgcn_rcpf(Ls);
      const float w0 = dpp<0x150>(wgt), w1 = dpp<0x151>(wgt), w2 = dpp<0x152>(wgt), w3 = dpp<0x153>(wgt);
      f32x4 a0 = z, a1 = z;
#pragma unroll
      for (int t = 0; t < 6; ++t) { a0 = mfma32(d[t], Aw[2 * t], a0); a1 = mfma32(d[t], Aw[2 * t + 1], a1); }
      const float y0 = fmaf(a0[0], w0, a0[1] * w1) + fmaf(a0[2], w2, a0[3] * w3), y1 = fmaf(a1[0], w0, a1[1] * w1) + fmaf(a1[2], w2, a1[3] * w3);
      auto sw = __builtin_amdgcn_permlane16_swap(__float_as_uint(y0), __float_as_uint(y1), false, false);
      const float yy = __uint_as_float(sw[0]) + __uint_as_float(sw[1]);
      auto sx = __builtin_amdgcn_permlane32_swap(__float_as_uint(yy), __float_as_uint(yy), false, false);
      *(LAS float*)(red + w * RED_ROW + (lane & 31) * 4) = __uint_as_float(sx[0]) + __uint_as_float(sx[1]);
      const bool ok = (d[0].w == T) & (d[1].w == T) & (d[2].w == T) & (d[3].w == T) & (d[4].w == T) & (d[5].w == T) & (d[6].w == T) & (d[7].w == T);
      RETRY(layer ? 8 : 1, OLOADS());
    }
    if (layer == 0) asm volatile("s_waitcnt vmcnt(0)" ::: "memory");
    LPOST(4 + 5 * layer);
    STAMP(4 + 12 * layer);
    if (w == TW) {
      f32x4 v;
      for (unsigned sp = 0; sp < (1u << 24); ++sp) {
        const v4u f = FL4(4 + 5 * layer);
        v = *(volatile LAS f32x4*)(red + ((lane >> 2) & 3) * RED_ROW + slot * 16);
        if (FL4OK(f)) break;
      }
#pragma unroll
      for (int k = 0; k < 4; ++k) { float t = v[k]; t += dpp<ROR8>(t); t += dpp<ROR4>(t); v[k] = t; }
      st_sc1(rs, yst, mkF(v[0], v[1], v[2], v[3], T));
    }
    if (layer == 1) {
      int dt = max((int)(d[6].z - X.tref), (int)(d[7].z - X.tref));
      dt = max(dt, (int)dppu<XOR1>((unsigned)dt)); dt = max(dt, (int)dppu<XOR2>((unsigned)dt)); dt = max(dt, (int)dppu<HMIR>((unsigned)dt)); dt = max(dt, (int)dppu<MIR>((unsigned)dt));
      RESYNC(X.tref + (unsigned)__builtin_amdgcn_readfirstlane(dt), 4, NOM_O);
      SLIP(5, 125);
    }
  }
  STAMP(5 + 12 * layer);
#define X_SHARE(xb, setx, sets) do { LPOST(setx); \
    float s1 = xr, s2 = xr * xr; \
    s1 += dpp<XOR1>(s1); s2 += dpp<XOR1>(s2); s1 += dpp<XOR2>(s1); s2 += dpp<XOR2>(s2); s1 += dpp<HMIR>(s1); s2 += dpp<HMIR>(s2); \
    *(LAS f32x2*)((xb) + 256 + (4 * w + kg) * 8) = f32x2{s1, s2}; LPOST(sets); } while (0)
#define X_FETCH(xb, setx) v2u a0, a1; for (unsigned sp = 0; sp < (1u << 24); ++sp) { const v4u f = FL4(setx); \
      a0 = *(volatile LAS v2u*)((xb) + (4 * kg + (i16 & 3)) * 8); a1 = *(volatile LAS v2u*)((xb) + 128 + (4 * kg + (i16 & 3)) * 8); if (FL4OK(f)) break; }
#define STATS_PUBLISH(xb, sets, idx) do { f32x2 sp2; for (unsigned sp = 0; sp < (1u << 24); ++sp) { const v4u f = FL4(sets); sp2 = *(volatile LAS f32x2*)((xb) + 256 + i16 * 8); if (FL4OK(f)) break; } \
    const float S1 = row_sum(sp2[0]), S2 = row_sum(sp2[1]); \
    st_sc1(rs, lane == 0 ? grp + (idx) + kq * 16 : OOB, v4u{__float_as_uint(S1), __float_as_uint(S2), rtc(), T}); } while (0)
  {
    const int go = kq * 2048 + (w >> 1) * 1024 + (8 * (c & 7) + 4 * (w & 1) + (i16 >> 2)) * 16;
    const bool gst = (lane & 0x33) == 0 && (c & 7) < 7;
    int goff = gst ? grp + GO_G + layer * O_L + go : OOB;
    int yoff = WS_Y + layer * 262144 + g * 32768 + (lane >> 3) * 2048 + (32 * kq + xun) * 16;
    PIN(goff); PIN(yoff);
    if (layer == 0) {
      {
        for (unsigned spins = 0;; ++spins) {
          if ((lane & 7) == g) fy0 = v2u{T, X.tref};
          if (__all(fy0.x == T) || spins > SPIN_CAP) break;
          asm volatile("" ::: "memory"); DELAY(14); PASS(3);
          fy0 = ld2_sc1(rs, WS_FY + (g * 8 + (lane & 7)) * 128);
        }
        int dt = (int)(fy0.y - X.t0); dt = max(dt, (int)dppu<XOR1>((unsigned)dt)); dt = max(dt, (int)dppu<XOR2>((unsigned)dt)); dt = max(dt, (int)dppu<HMIR>((unsigned)dt));
        X.tref = X.t0 + (unsigned)__builtin_amdgcn_readfirstlane(dt);
      }
      if (DIAG == 1) X.dv = X.tref - X.t0;
      { unsigned t_ = X.tref + (unsigned)DLV(1); for (int k_ = 0; k_ < 4096 && (int)(rtc() - t_) < 0; ++k_) {} }
    } else WAIT_AT(X.tref, 6);
    LAS unsigned char* xb = L + L_XB + 2 * layer * XB_SZ;
    float xn;
    v4u r0 = ld_sc1(rs, yoff), r1 = ld_sc1(rs, yoff + 16384);
    SCHED();
    for (unsigned spins = 0;; ++spins) {
      const v4u d0 = unF(r0, T), d1 = unF(r1, T);
      float t = rows_reduce4(__builtin_bit_cast(f32x4, d0) + __builtin_bit_cast(f32x4, d1));
      t += dpp<ROR8>(t);
      const unsigned p = pk(xr + t, 0.f);
      *(LAS u16*)(xb + (4 * xun + kg) * 2) = (u16)p;
      xn = bflo(p);
      const bool ok = ((or4(d0) | or4(d1)) & 0xffu) == 0u;
      RETRY(layer ? 9 : 4, r0 = ld_sc1(rs, yoff); r1 = ld_sc1(rs, yoff + 16384));
    }
    xr = xn;
    STAMP(6 + 12 * layer);
    X_SHARE(xb, 6 + 4 * layer, 12 + layer);
    if ((c & 7) < 7) {
      X_FETCH(xb, 6 + 4 * layer);
      const float part = gemv128(a0, a1, Bg);
      const float p1 = dpp<0x55>(part), p2 = dpp<0xAA>(part), p3 = dpp<0xFF>(part);
      st_sc1(rs, goff, mkF(part, p1, p2, p3, T));
    } else if (w == 0) STATS_PUBLISH(xb, 12 + layer, GO_ST + layer * O_L);
  }
  if (layer == 1) STAMP(19);
  SLIP(layer == 0 ? 1 : 6, 98);
  if (layer == 0) {
    const v4u zz = {0u, 0u, 0u, 0u};
    const int slot = ((lane >> 4) & 1) * 4 + (lane & 3), rep = ((lane >> 2) & 3) + 4 * (lane >> 5);
    if (w == TW) {
      st_sc1(rs, WS_Y + 262144 + rep * 32768 + (2 * g + (c & 1)) * 2048 + (c >> 1) * 128 + slot * 16, zz);
      st_sc1(rs, i16 < 8 ? WS_X + 131072 + i16 * 16384 + g * 2048 + c * 64 + kg * 16 : OOB, zz);
    }
    st_sc1(rs, lane < 30 ? grp + O_L + (c * 4 + w) * 480 + lane * 16
                         : ((lane & 0x33) == 0x20 ? grp + GO_Q1 + kq * 2048 + ((c & 7) * 64 + w * 16 + i16) * 4 : ((lane == 63 && (c & 7) == 0 && w == 0) ? grp + GO_ST2 + kq * 16 : OOB)), zz);
  }
  {
    const int hu = 14 * w + (i16 < 14 ? i16 : 13);
    const int gi = kg * 2048 + hu * 16;
    int gld = grp + GO_G + layer * O_L + gi, soff = grp + GO_ST + layer * O_L + (lane & 3) * 16;
    int xst = i16 < 8 ? WS_X + layer * 131072 + i16 * 16384 + g * 2048 + c * 64 + kg * 16 : OOB;
    PIN(gld); PIN(soff); PIN(xst);
    if (layer == 0 && w == TW) {
      LWAIT4(15);
#pragma unroll
      for (int t = 0; t < 7; ++t) {
        const int r0 = 32 * t + 8 * kg + (i16 >> 2);
        const v2u lo = trread(L + L_IDN + r0 * 32 + (i16 & 3) * 8), hi = trread(L + L_IDN + (r0 + 4) * 32 + (i16 & 3) * 8);
        Bd[t] = v4u{lo.x, lo.y, hi.x, hi.y};
      }
    }
    WAIT_AT(X.tref, layer == 0 ? 2 : 7);
    LAS unsigned char* hb = L + L_HB + layer * 512;
    v4u ds, r0, r1;
#define GLOADS() do { ds = ld_sc1(rs, soff); if (layer == 0) { c0 = ld_sc1(rs, WS_GC + g * 8192 + gi); c1 = ld_sc1(rs, WS_GC + g * 8192 + gi + 1024); } \
      r0 = ld_sc1(rs, gld); r1 = ld_sc1(rs, gld + 1024); } while (0)
    GLOADS();
    SCHED();
    for (unsigned spins = 0;; ++spins) {
      LN_STATS(ds);
      unsigned cbad = 0u;
      if (layer == 0) {
        const v4u e0 = unF(c0, T), e1 = unF(c1, T); cbad = or4(e0) | or4(e1);
        csg[0] = rows_reduce4(__builtin_bit_cast(f32x4, e0)); csg[1] = rows_reduce4(__builtin_bit_cast(f32x4, e1));
      }
      const v4u d0 = unF(r0, T), d1 = unF(r1, T);
      const float gv = rstd * fmaf(-mean, csg[0], rows_reduce4(__builtin_bit_cast(f32x4, d0)));
      const float uv = rstd * fmaf(-mean, csg[1], rows_reduce4(__builtin_bit_cast(f32x4, d1)));
      const float hv = uv * __builtin_amdgcn_rcpf(1.f + __builtin_amdgcn_exp2f(-1.4426950408889634f * gv));
      *(LAS u16*)(hb + (4 * hu + kg) * 2) = (u16)pk(hv, 0.f);
      const bool ok = (((or4(d0) | or4(d1) | cbad) & 0xffu) == 0u) & (ds.w == T);
      RETRY(5, GLOADS());
    }
    if (layer == 0) asm volatile("s_waitcnt vmcnt(0)" ::: "memory");
    LPOST(7 + 4 * layer);
    STAMP(8 + 12 * layer);
    if (w == TW) {
      v4u hf[7];
      for (unsigned sp = 0; sp < (1u << 24); ++sp) {
        const v4u f = FL4(7 + 4 * layer);
#pragma unroll
        for (int t = 0; t < 7; ++t) hf[t] = *(volatile LAS v4u*)(hb + t * 64 + kg * 16);
        if (FL4OK(f)) break;
      }
      f32x4 a0 = z, a1 = z;
#pragma unroll
      for (int t = 0; t < 7; ++t) { if (t & 1) a1 = mfma32(Bd[t], hf[t], a1); else a0 = mfma32(Bd[t], hf[t], a0); }
      a0 += a1;
      st_sc1(rs, xst, mkF(a0[0], a0[1], a0[2], a0[3], T));
    }
    if (layer == 0) STAMP(10);
    RESYNC(QUAD_TMAX(ds.z), layer == 0 ? 1 : 6, NOM_G);
    SLIP(layer == 0 ? 2 : 7, layer == 0 ? 125 : 105);
  }
  if (layer == 0 || (g == 0 && (c & 7) == 0)) {
    int xoff = WS_X + layer * 131072 + g * 16384 + (lane >> 3) * 2048 + (32 * kq + xun) * 16;
    int qst = (lane & 0x33) == 0 ? grp + GO_Q1 + kq * 2048 + ((c & 7) * 64 + w * 16 + i16) * 4 : OOB;
    PIN(xoff); PIN(qst);
    WAIT_AT(X.tref, layer == 0 ? 3 : 9);
    LAS unsigned char* xb = L + L_XB + XB_SZ;
    unsigned p;
    v4u r0 = ld_sc1(rs, xoff);
    for (unsigned spins = 0;; ++spins) {
      const v4u d0 = unF(r0, T);
      float t = rows_reduce4(__builtin_bit_cast(f32x4, d0));
      t += dpp<ROR8>(t);
      p = pk(xr + t, 0.f);
      if (layer == 0) *(LAS u16*)(xb + (4 * xun + kg) * 2) = (u16)p;
      const bool ok = (or4(d0) & 0xffu) == 0u;
      RETRY(6, r0 = ld_sc1(rs, xoff));
    }
    if (layer == 0) {
      xr = bflo(p);
      X_SHARE(xb, 8, 14);
      X_FETCH(xb, 8);
      const float part = gemv128(a0, a1, Bq);
      const float p1 = dpp<0x55>(part), p2 = dpp<0xAA>(part), p3 = dpp<0xFF>(part);
      st_sc1(rs, qst, mkF(part, p1, p2, p3, T));
      if ((c & 7) == 0 && w == 0) STATS_PUBLISH(xb, 14, GO_ST2);
      STAMP(11);
      SLIP(3, 100);
    } else {
      if ((lane & 8) == 0) *(GAS u16*)(X.out + 128 * kq + 4 * xun + kg) = (u16)p;
      if (c == 0 && w == 0 && lane == 0) st1_sc1(rs, 0, X.E + 1u);
      if (c == 0 && w == 0) { if (DIAG >= 5) X.dv = __builtin_amdgcn_readfirstlane(*(volatile LAS unsigned*)(L + L_FL + 11 * 16 + 256 + 2048)) - X.t0; const unsigned e_ = rtc() + X.dv; for (int k_ = 0; k_ < 100000 && (int)(rtc() - e_) < 0; ++k_) {} }
      STAMP(21);
    }
  }
}

__global__ void __launch_bounds__(512, 2)
fused_decode(const u16* __restrict__ x, const u16* __restrict__ kc, const u16* __restrict__ vc, const u16* __restrict__ wq,
             const u16* __restrict__ wo, const u16* __restrict__ wg, const u16* __restrict__ wu, const u16* __restrict__ wd,
             u16* __restrict__ out, unsigned char* __restrict__ ws) {
  extern __shared__ __attribute__((aligned(16))) unsigned char smem[];
  Ctx X;
  X.vc = vc; X.wo = wo; X.wg = wg; X.wu = wu; X.wd = wd; X.out = out; X.ws = ws;
  X.L = (LAS unsigned char*)smem;
  const int tid = threadIdx.x;
  X.lane = tid & 63; X.wv = __builtin_amdgcn_readfirstlane(tid >> 6); X.w = X.wv & 3; X.kg = X.lane >> 4; X.i16 = X.lane & 15;
  X.b = blockIdx.x; X.g = X.b & 7; X.c = X.b >> 3;
  X.rs = __builtin_amdgcn_make_buffer_rsrc((void*)ws, 0, WS_BYTES, 0x00020000);
  STAMP(0);
  X.t0 = rtc(); X.dv = 0;
  const int lane = X.lane, w = X.w, kg = X.kg, i16 = X.i16, g = X.g, c = X.c, kq = c >> 3;
  const __amdgpu_buffer_rsrc_t rs = X.rs; LAS unsigned char* L = X.L;
  unsigned pfv;
  {
    unsigned long long pc; unsigned len;
    asm volatile("s_getpc_b64 %0\n\tLpf_here:\n\ts_mov_b32 %1, Lpf_end - Lpf_here" : "=s"(pc), "=s"(len));
    unsigned off = (unsigned)(c * 8 + X.wv) * 128u; off = off < len ? off : 0u;
    pfv = *(const GAS unsigned*)(pc + off);
  }
  SCHED();
  asm volatile("" :: "s"(x), "s"(kc), "s"(vc), "s"(wq), "s"(wo), "s"(wg), "s"(wu), "s"(wd), "s"(out), "s"(ws));
  v4u Kf[8], Vf[8], Sw[10], wr[4], xf; v2u xa, xb; unsigned xu, E;
  if (X.wv >= 4) {
    DELAY(12);
    const size_t kvbase = ((size_t)g * SK + (SK - WIN) + 128 * c + 32 * w) * DH;
#pragma unroll
    for (int s = 0; s < 8; ++s) Kf[s] = ldg_nt(kc + kvbase + s * 512 + lane * 8);
#pragma unroll
    for (int j = 0; j < VPRE; ++j) Vf[j] = ldg_nt(vc + kvbase + (16 * (j >> 2) + 4 * kg + (j & 3)) * DH + 8 * i16);
  } else {
#pragma unroll
    for (int n = 0; n < 4; ++n) wr[n] = ldg_nt(wq + (size_t)(128 * kq + 32 * w + 8 * n + (lane >> 3)) * 4096 + 512 * g + 64 * (c & 7) + 8 * (lane & 7));
    xf = *(const GAS v4u*)(x + 8 * lane);
    xa = *(const GAS v2u*)(x + 4 * (32 * kq + 4 * kg + (i16 & 3))); xb = *(const GAS v2u*)(x + 4 * (32 * kq + 16 + 4 * kg + (i16 & 3)));
    xu = *(const GAS u16*)(x + 128 * kq + 4 * (8 * w + (lane & 7)) + kg);
    E = ld1_sc1(rs, 0);
    SCHED();
    {
      const v4u zz = {0u, 0u, 0u, 0u};
      const int slot = ((lane >> 4) & 1) * 4 + (lane & 3), rep = ((lane >> 2) & 3) + 4 * (lane >> 5), grp = WS_GRP + g * GRP_SZ;
      if (w == TW) {
        st_sc1(rs, WS_Y + rep * 32768 + (2 * g + (c & 1)) * 2048 + (c >> 1) * 128 + slot * 16, zz);
        st_sc1(rs, i16 < 8 ? WS_X + i16 * 16384 + g * 2048 + c * 64 + kg * 16 : OOB, zz);
      }
      st_sc1(rs, (c & 7) < 7 ? ((lane & 0x33) == 0 ? grp + GO_G + kq * 2048 + (w >> 1) * 1024 + (8 * (c & 7) + 4 * (w & 1) + (i16 >> 2)) * 16 : OOB)
                             : ((w == 0 && lane == 0) ? grp + GO_ST + kq * 16 : OOB), zz);
    }
  }
  SCHED();
  int wv2 = X.wv; asm volatile("" : "+s"(wv2));
  if (wv2 >= 4) run_A(X, Kf, Vf, Sw);
  int wv3 = X.wv; asm volatile("" : "+s"(wv3));
  if (wv3 < 4) {
#pragma unroll
    for (int n = 0; n < 4; ++n) *(LAS v4u*)(L + L_IMG + w * 4096 + n * 1024 + lane * 16) = wr[n];
    const unsigned hsh = (E + 1u) * 0x9E3779B1u;
    const unsigned T = __builtin_amdgcn_readfirstlane(((hsh & 0xbfffbf00u) | (((hsh >> 8) & 0xbfu) ^ 0xa5u)));
    X.E = E; X.T = T;
    LPOST(0);
    float xr = bflo(xu);
    v2u Bq[8], Bg[8]; v4u Aw[12], Bd[7];
    {
      float xv[8] = {bflo(xf.x), bfhi(xf.x), bflo(xf.y), bfhi(xf.y), bflo(xf.z), bfhi(xf.z), bflo(xf.w), bfhi(xf.w)};
      float s = 0.f;
#pragma unroll
      for (int k = 0; k < 8; ++k) s += xv[k];
      const float mean = xrow_sum(row_sum(s)) * (1.f / DM);
      float v = 0.f;
#pragma unroll
      for (int k = 0; k < 8; ++k) { const float d = xv[k] - mean; v = fmaf(d, d, v); }
      const float var = xrow_sum(row_sum(v)) * (1.f / DM);
      const float mb = rbf(mean), isd = 1.0f / rbf(sqrtf(rbf(rbf(var) + 1e-5f)));
      const float xi[8] = {bflo(xa.x), bfhi(xa.x), bflo(xa.y), bfhi(xa.y), bflo(xb.x), bfhi(xb.x), bflo(xb.y), bfhi(xb.y)};
      float xn[8];
#pragma unroll
      for (int k = 0; k < 8; ++k) xn[k] = rbf(xi[k] - mb) * isd;
      const v2u a0 = {pk(xn[0], xn[1]), pk(xn[2], xn[3])}, a1 = {pk(xn[4], xn[5]), pk(xn[6], xn[7])};
      LWAIT4(0);
#pragma unroll
      for (int u = 0; u < 8; ++u) Bq[u] = trread(L + L_IMG + (64 * (u >> 2) + 16 * kg + 4 * (u & 3) + (i16 >> 2)) * 128 + w * 32 + (i16 & 3) * 8);
      const int qo = (lane & 0x33) == 0 ? WS_Q0 + g * 8192 + kq * 2048 + ((c & 7) * 64 + w * 16 + i16) * 4 : OOB - 65536;
      {
        const float part = gemv128(a0, a1, Bq);
        const float p1 = dpp<0x55>(part), p2 = dpp<0xAA>(part), p3 = dpp<0xFF>(part);
        st_sc1(rs, qo, mkF(part, p1, p2, p3, T));
      }
      STAMP(1);
      if (DIAG == 2) X.dv = rtc() - X.t0;
      {
        const v2u one = {ONES, ONES};
        const float part = gemv128(one, one, Bq);
        const float p1 = dpp<0x55>(part), p2 = dpp<0xAA>(part), p3 = dpp<0xFF>(part);
        st_sc1(rs, qo + 65536, mkF(part, p1, p2, p3, T));
      }
    }
    f32x4 csq = {0.f, 0.f, 0.f, 0.f}; float csg[2] = {0.f, 0.f};
    run_B<0>(X, Bq, Aw, Bg, Bd, xr, csq, csg);
    run_B<1>(X, Bq, Aw, Bg, Bd, xr, csq, csg);
  }
  asm volatile("" :: "v"(pfv));
  STAMP_FLUSH();
  asm volatile("Lpf_end:");
}

extern "C" void kernel_launch(void* const* d_in, const int* in_sizes, int n_in, void* d_out, int out_size, void* d_ws, size_t ws_size,
                              hipStream_t stream) {
  (void)in_sizes; (void)n_in; (void)out_size; (void)ws_size;
  (void)hipFuncSetAttribute((const void*)fused_decode, hipFuncAttributeMaxDynamicSharedMemorySize, LDS_BYTES);
  hipLaunchKernelGGL(fused_decode, dim3(256), dim3(512), LDS_BYTES, stream, (const u16*)d_in[0], (const u16*)d_in[1], (const u16*)d_in[2],
                     (const u16*)d_in[3], (const u16*)d_in[4], (const u16*)d_in[6], (const u16*)d_in[7], (const u16*)d_in[8], (u16*)d_out,
                     (unsigned char*)d_ws);
}
```

```cpp
#include <hip/hip_runtime.h>
#include <stdint.h>

typedef unsigned short u16;
typedef unsigned v4u __attribute__((ext_vector_type(4)));
typedef unsigned v2u __attribute__((ext_vector_type(2)));
typedef float f32x4 __attribute__((ext_vector_type(4)));
typedef float f32x2 __attribute__((ext_vector_type(2)));
typedef short s16x4 __attribute__((ext_vector_type(4)));
typedef __bf16 bf16x8 __attribute__((ext_vector_type(8)));
typedef __bf16 bf16x2 __attribute__((ext_vector_type(2)));
#define LAS __attribute__((address_space(3)))
#define GAS __attribute__((address_space(1)))

constexpr int SK = 32768, WIN = 4096, DH = 128, DM = 512, FF = 1792;
constexpr int WS_FO = 4096;
constexpr int WS_FY = WS_FO + 32768;
constexpr int WS_X = WS_FY + 8192;
constexpr int WS_Y = WS_X + 262144;
constexpr int WS_Q0 = WS_Y + 524288;
constexpr int WS_GC = WS_Q0 + 131072;
constexpr int WS_GRP = WS_GC + 65536, GRP_SZ = 143360, O_L = 61440;
constexpr int GO_ML = 0;
constexpr int GO_O = 2048;
constexpr int GO_G = 51200;
constexpr int GO_ST = 59392;
constexpr int GO_ST2 = O_L + 61440 + 8192;
constexpr int GO_Q1 = O_L + 61440;
static_assert(GO_ST2 + 128 <= GRP_SZ && 128 * 480 == O_L, "group block");
constexpr int WS_BYTES = WS_GRP + 8 * GRP_SZ;
constexpr int OOB = 0x7fffff00;
constexpr int WS_STAMP = 8 << 20;
constexpr int L_IMG = 0;
constexpr int L_IGU = 16384;
constexpr int L_IDN = 32768;
constexpr int L_IWO = 40960;
constexpr int L_STG = 57344, STG_W = 8704;
constexpr int L_ATT = L_STG + 4 * STG_W;
constexpr int L_ML = L_ATT + 8192;
constexpr int L_QB = L_ML + 128, QSTR = 320;
constexpr int L_XB = L_QB + 4 * QSTR;
constexpr int XB_SZ = 384;
constexpr int L_HB = L_XB + 3 * XB_SZ;
constexpr int L_FL = L_HB + 1024;
constexpr int L_RED = L_FL + 256, RED_ROW = 144, RED_SZ = 4 * RED_ROW;
constexpr int LDS_BYTES = 112 * 1024;
static_assert(L_RED + 2 * RED_SZ <= LDS_BYTES, "lds");
constexpr int TW = 3;
constexpr unsigned SPIN_CAP = 1u << 17;

constexpr int DL[16] = {  105,   290,   420,   555,   675,   825,   970,   1090,
                          3,   1220,   1,   400,   20,
                          15,   4,   40};

constexpr int NOM_G = 85, NOM_Q = 92, NOM_O = 120;
#define SCHED() __builtin_amdgcn_sched_barrier(0)
#define STAMP(i) do {} while (0)
#define PASS(i) do {} while (0)
#define STAMP_FLUSH() do {} while (0)
#define DELAY(i) do { if (DL[i] > 0) __builtin_amdgcn_s_sleep(DL[i]); } while (0)
#define DLV(i) DL[i]
__device__ __forceinline__ unsigned rtc() { return (unsigned)__builtin_amdgcn_s_memrealtime(); }
#define WAIT_AT(ref, i) do { SCHED(); unsigned tg_ = (ref) + (unsigned)DLV(i); const unsigned lo_ = rtc() + (unsigned)DLV(13); if ((int)(lo_ - tg_) > 0) tg_ = lo_; \
    for (int k_ = 0; k_ < 4096 && (int)(rtc() - tg_) < 0; ++k_) {} SCHED(); } while (0)
#define PW_A 0
#define VPRE 0
#define DN_LATE 1
#define PW_B 1
#define UNLIKELY(x) __builtin_expect(!!(x), 0)
#define LIKELY(x) __builtin_expect(!!(x), 1)
#define PIN(v) asm volatile("" : "+v"(v))

__device__ __forceinline__ float bflo(unsigned u) { return __uint_as_float(u << 16); }
__device__ __forceinline__ float bfhi(unsigned u) { return __uint_as_float(u & 0xffff0000u); }
__device__ __forceinline__ unsigned pk(float lo, float hi) { bf16x2 p = {(__bf16)lo, (__bf16)hi}; return __builtin_bit_cast(unsigned, p); }
__device__ __forceinline__ float rbf(float f) { return bflo((unsigned)__builtin_bit_cast(u16, (__bf16)f)); }
__device__ __forceinline__ float vmax(float a, float b) { return __builtin_elementwise_maximum(a, b); }
template <int CTRL> __device__ __forceinline__ float dpp(float x) {
  return __builtin_bit_cast(float, __builtin_amdgcn_mov_dpp(__builtin_bit_cast(int, x), CTRL, 0xf, 0xf, true));
}
template <int CTRL> __device__ __forceinline__ unsigned dppu(unsigned x) { return (unsigned)__builtin_amdgcn_mov_dpp((int)x, CTRL, 0xf, 0xf, true); }
constexpr int XOR1 = 0xB1, XOR2 = 0x4E, HMIR = 0x141, MIR = 0x140, ROR4 = 0x124, ROR8 = 0x128;
__device__ __forceinline__ float row_sum(float v) { v += dpp<XOR1>(v); v += dpp<XOR2>(v); v += dpp<HMIR>(v); v += dpp<MIR>(v); return v; }
__device__ __forceinline__ float row_max(float v) { v = vmax(v, dpp<XOR1>(v)); v = vmax(v, dpp<XOR2>(v)); v = vmax(v, dpp<HMIR>(v)); v = vmax(v, dpp<MIR>(v)); return v; }
__device__ __forceinline__ float xrow_sum(float x) {
  auto s = __builtin_amdgcn_permlane16_swap(__float_as_uint(x), __float_as_uint(x), false, false);
  x = __uint_as_float(s[0]) + __uint_as_float(s[1]);
  auto t = __builtin_amdgcn_permlane32_swap(__float_as_uint(x), __float_as_uint(x), false, false);
  return __uint_as_float(t[0]) + __uint_as_float(t[1]);
}
__device__ __forceinline__ float xrow_max(float x) {
  auto s = __builtin_amdgcn_permlane16_swap(__float_as_uint(x), __float_as_uint(x), false, false);
  x = vmax(__uint_as_float(s[0]), __uint_as_float(s[1]));
  auto t = __builtin_amdgcn_permlane32_swap(__float_as_uint(x), __float_as_uint(x), false, false);
  return vmax(__uint_as_float(t[0]), __uint_as_float(t[1]));
}
__device__ __forceinline__ float rows_reduce4(f32x4 v) {
  auto a = __builtin_amdgcn_permlane16_swap(__float_as_uint(v[0]), __float_as_uint(v[1]), false, false);
  auto b = __builtin_amdgcn_permlane16_swap(__float_as_uint(v[2]), __float_as_uint(v[3]), false, false);
  const float t01 = __uint_as_float(a[0]) + __uint_as_float(a[1]), t23 = __uint_as_float(b[0]) + __uint_as_float(b[1]);
  auto c = __builtin_amdgcn_permlane32_swap(__float_as_uint(t01), __float_as_uint(t23), false, false);
  return __uint_as_float(c[0]) + __uint_as_float(c[1]);
}
__device__ __forceinline__ f32x4 mfma32(v4u a, v4u b, f32x4 c) {
  return __builtin_amdgcn_mfma_f32_16x16x32_bf16(__builtin_bit_cast(bf16x8, a), __builtin_bit_cast(bf16x8, b), c, 0, 0, 0);
}
__device__ __forceinline__ f32x4 mfma16(v2u a, v2u b, f32x4 c) {
  return __builtin_amdgcn_mfma_f32_16x16x16bf16_1k(__builtin_bit_cast(s16x4, a), __builtin_bit_cast(s16x4, b), c, 0, 0, 0);
}
__device__ __forceinline__ float dot2(unsigned a, unsigned b, float c) {
  return __builtin_amdgcn_fdot2_f32_bf16(__builtin_bit_cast(bf16x2, a), __builtin_bit_cast(bf16x2, b), c, false);
}
__device__ __forceinline__ v2u trread(LAS unsigned char* p) { return __builtin_bit_cast(v2u, __builtin_amdgcn_ds_read_tr16_b64_v4i16((LAS s16x4*)p)); }
__device__ __forceinline__ v4u ldg_nt(const u16* p) { return __builtin_nontemporal_load((const GAS v4u*)p); }
__device__ __forceinline__ v4u ld_sc1(__amdgpu_buffer_rsrc_t rs, int off) { return __builtin_amdgcn_raw_buffer_load_b128(rs, off, 0, 16); }
__device__ __forceinline__ unsigned ld1_sc1(__amdgpu_buffer_rsrc_t rs, int off) { return __builtin_amdgcn_raw_buffer_load_b32(rs, off, 0, 16); }
__device__ __forceinline__ v2u ld2_sc1(__amdgpu_buffer_rsrc_t rs, int off) { return __builtin_amdgcn_raw_buffer_load_b64(rs, off, 0, 16); }
__device__ __forceinline__ void st2_sc1(__amdgpu_buffer_rsrc_t rs, int off, v2u v) { __builtin_amdgcn_raw_buffer_store_b64(v, rs, off, 0, 16); }
__device__ __forceinline__ void st_sc1(__amdgpu_buffer_rsrc_t rs, int off, v4u v) { __builtin_amdgcn_raw_buffer_store_b128(v, rs, off, 0, 16); }
__device__ __forceinline__ void st1_sc1(__amdgpu_buffer_rsrc_t rs, int off, unsigned v) { __builtin_amdgcn_raw_buffer_store_b32(v, rs, off, 0, 16); }
#define PERM(hi, lo, sel) __builtin_amdgcn_perm((hi), (lo), (sel))
__device__ __forceinline__ bool okH(v4u d, unsigned T) { return (PERM(d.y, d.x, 0x05040100u) == T) & (PERM(d.w, d.z, 0x05040100u) == T); }
__device__ __forceinline__ v2u valH(v4u d) { return v2u{PERM(d.y, d.x, 0x07060302u), PERM(d.w, d.z, 0x07060302u)}; }
__device__ __forceinline__ v4u mkH(unsigned p0, unsigned p1, unsigned T) {
  return v4u{PERM(p0, T, 0x05040100u), PERM(p0, T, 0x07060302u), PERM(p1, T, 0x05040100u), PERM(p1, T, 0x07060302u)};
}
__device__ __forceinline__ v4u unF(v4u d, unsigned T) { return v4u{d.x ^ (T & 0xffu), d.y ^ ((T >> 8) & 0xffu), d.z ^ ((T >> 16) & 0xffu), d.w ^ (T >> 24)}; }
__device__ __forceinline__ unsigned or4(v4u d) { return (d.x | d.y) | (d.z | d.w); }
__device__ __forceinline__ v4u mkF(float a, float b, float c, float d, unsigned T) {
  return v4u{PERM(__float_as_uint(a), T, 0x07060500u), PERM(__float_as_uint(b), T, 0x07060501u),
             PERM(__float_as_uint(c), T, 0x07060502u), PERM(__float_as_uint(d), T, 0x07060503u)};
}
#define LGKM0() asm volatile("s_waitcnt lgkmcnt(0)" ::: "memory")
#define BARRIER() do { LGKM0(); __builtin_amdgcn_s_barrier(); asm volatile("" ::: "memory"); } while (0)

__device__ __forceinline__ float gemv128(v2u a0, v2u a1, const v2u (&B)[8]) {
  const f32x4 z = {0.f, 0.f, 0.f, 0.f};
  f32x4 c0 = mfma16(a0, B[0], z), c1 = mfma16(a0, B[1], z), c2 = mfma16(a0, B[2], z), c3 = mfma16(a0, B[3], z);
  c0 = mfma16(a1, B[4], c0); c1 = mfma16(a1, B[5], c1); c2 = mfma16(a1, B[6], c2); c3 = mfma16(a1, B[7], c3);
  return (c0[0] + c1[1]) + (c2[2] + c3[3]);
}
struct Ctx {
  const u16 *vc, *wo, *wg, *wu, *wd; u16* out; unsigned char* ws;
  __amdgpu_buffer_rsrc_t rs; LAS unsigned char* L;
  int lane, w, wv, kg, i16, b, g, c; unsigned E, T, t0, tref;
};

#define LPOST(set) do { asm volatile("" ::: "memory"); *(volatile LAS unsigned*)(L + L_FL + (set) * 16 + w * 4) = T; asm volatile("" ::: "memory"); } while (0)
#define LWAIT4(set) do { for (unsigned spins_ = 0; spins_ < (1u << 24); ++spins_) { const v4u f_ = *(volatile LAS v4u*)(L + L_FL + (set) * 16); \
      if ((f_.x == T) & (f_.y == T) & (f_.z == T) & (f_.w == T)) break; } asm volatile("" ::: "memory"); } while (0)
#define LWAIT1(set, who) do { for (unsigned spins_ = 0; spins_ < (1u << 24); ++spins_) { \
      if (*(volatile LAS unsigned*)(L + L_FL + (set) * 16 + (who) * 4) == T) break; __builtin_amdgcn_s_sleep(1); } asm volatile("" ::: "memory"); } while (0)
#define ONES 0x3f803f80u
#define FL4(set) (*(volatile LAS v4u*)(L + L_FL + (set) * 16))
#define FL4OK(f) (((f).x == T) & ((f).y == T) & ((f).z == T) & ((f).w == T))

__device__ __forceinline__ v4u load_sw(const Ctx& X, int n) {
  const int cb = (X.c & 7) < 7 ? (X.c & 7) : 6;
  if (n < 4) return ldg_nt(X.wo + (size_t)(512 * X.g + 256 * (X.c & 1) + 64 * X.w + 16 * n + (X.lane >> 2)) * DM + 32 * (X.c >> 1) + 8 * (X.lane & 3));
  if (n < 8) return ldg_nt((n >= 6 ? X.wu : X.wg) + (size_t)(128 * (X.c >> 3) + 32 * X.w + 16 * (n & 1) + (X.lane >> 2)) * FF + 224 * X.g + 32 * cb + 8 * (X.lane & 3));
  const int blk = 2 * X.w + (n & 1) < 7 ? 2 * X.w + (n & 1) : 6;
  return ldg_nt(X.wd + (size_t)(224 * X.g + 32 * blk + (X.lane >> 1)) * DM + 16 * X.c + 8 * (X.lane & 1));
}
__device__ __forceinline__ void run_A(Ctx& X, v4u (&Kf)[8], v4u (&Vf)[8], v4u (&Sw)[10]) {
  const int lane = X.lane, w = X.w, kg = X.kg, i16 = X.i16, g = X.g, c = X.c;
  const __amdgpu_buffer_rsrc_t rs = X.rs; LAS unsigned char* L = X.L; LAS unsigned char* stg = L + L_STG + w * STG_W;
  const f32x4 z = {0.f, 0.f, 0.f, 0.f};
  STAMP(12);
  const int u = (lane * 43) >> 7;
  const bool head3 = lane == 3 * u;
  int ooff = head3 ? WS_GRP + g * GRP_SZ + GO_O + w * 12288 + u * 512 + c * 16 : (lane == 1 ? WS_GRP + g * GRP_SZ + GO_ML + w * 512 + c * 16 : OOB);
  const int ostep = (head3 || lane == 1) ? O_L : 0;
  const u16* vb = X.vc + ((size_t)g * SK + (SK - WIN) + 128 * c + 32 * w) * DH;
  {
#pragma unroll
    for (int s = 0; s < 8; ++s) {
      *(LAS v4u*)(stg + (4 * s + kg) * 272 + i16 * 16) = Kf[s];
      SCHED();
      if (s + VPRE < 8) { const int j = s + VPRE; Vf[j] = ldg_nt(vb + (16 * (j >> 2) + 4 * kg + (j & 3)) * DH + 8 * i16); }
      SCHED();
    }
    LGKM0();
#pragma unroll
    for (int s = 0; s < 8; ++s) Kf[s] = *(LAS v4u*)(stg + (16 * (s >> 2) + i16) * 272 + (s & 3) * 64 + kg * 16);
  }
  {
    v4u t[8];
#pragma unroll
    for (int jj = 0; jj < 4; ++jj) {
#pragma unroll
      for (int n = 0; n < 8; ++n) t[n][jj] = PERM(Vf[2 * jj + 1][n >> 1], Vf[2 * jj][n >> 1], (n & 1) ? 0x07060302u : 0x05040100u);
      asm volatile("" : "+v"(t[0][jj]), "+v"(t[7][jj]));
      SCHED();
      Sw[jj] = load_sw(X, jj);
      SCHED();
    }
#pragma unroll
    for (int n = 0; n < 8; ++n) Vf[n] = t[n];
    asm volatile("" :: "v"(Vf[0]), "v"(Vf[1]), "v"(Vf[2]), "v"(Vf[3]), "v"(Vf[4]), "v"(Vf[5]), "v"(Vf[6]), "v"(Vf[7]));
  }
  STAMP(13);
#pragma unroll
  for (int n = 0; n < 4; ++n) { *(LAS v4u*)(L + L_IWO + w * 4096 + n * 1024 + lane * 16) = Sw[n]; SCHED(); Sw[4 + n] = load_sw(X, 4 + n); SCHED(); }
#pragma unroll
  for (int n = 0; n < 4; ++n) {
    *(LAS v4u*)(L + L_IGU + (n >> 1) * 8192 + w * 2048 + (n & 1) * 1024 + lane * 16) = Sw[4 + n]; SCHED();
    if (n & 1) { Sw[8 + (n >> 1)] = load_sw(X, 8 + (n >> 1)); SCHED(); }
  }
  if (DN_LATE == 0) {
#pragma unroll
    for (int n = 0; n < 2; ++n) *(LAS v4u*)(L + L_IDN + (2 * w + n) * 1024 + lane * 16) = Sw[8 + n];
  }
  STAMP(9);
#pragma unroll
  for (int layer = 0; layer < 2; ++layer) {
    BARRIER();
    const unsigned T = __builtin_amdgcn_readfirstlane(*(volatile LAS unsigned*)(L + L_FL));
    {
      v4u qf[4];
#pragma unroll
      for (int s = 0; s < 4; ++s) qf[s] = *(LAS v4u*)(L + L_QB + (i16 >> 2) * QSTR + s * 64 + kg * 16);
      f32x4 sa = z, sb = z;
#pragma unroll
      for (int s = 0; s < 4; ++s) { sa = mfma32(Kf[s], qf[s], sa); sb = mfma32(Kf[4 + s], qf[s], sb); }
      float sv[8];
      if (layer == 0) {
#pragma unroll
        for (int k = 0; k < 8; k += 2) {
          const unsigned p = k < 4 ? pk(sa[k & 3], sa[(k & 3) + 1]) : pk(sb[k & 3], sb[(k & 3) + 1]);
          const unsigned r = pk(bflo(p) * 0.08838834764831845f, bfhi(p) * 0.08838834764831845f);
          sv[k] = bflo(r); sv[k + 1] = bfhi(r);
        }
      } else {
#pragma unroll
        for (int k = 0; k < 4; ++k) { sv[k] = sa[k]; sv[4 + k] = sb[k]; }
      }
      const float CE = layer == 0 ? 1.4426950408889634f : 1.4426950408889634f * 0.08838834764831845f;
      const float m = xrow_max(vmax(vmax(vmax(sv[0], sv[1]), vmax(sv[2], sv[3])), vmax(vmax(sv[4], sv[5]), vmax(sv[6], sv[7])))) * CE;
#pragma unroll
      for (int k = 0; k < 8; ++k) sv[k] = __builtin_amdgcn_exp2f(fmaf(sv[k], CE, -m));
      const v4u pf = {pk(sv[0], sv[1]), pk(sv[2], sv[3]), pk(sv[4], sv[5]), pk(sv[6], sv[7])};
      f32x4 o[8];
#pragma unroll
      for (int n = 0; n < 8; ++n) o[n] = mfma32(pf, Vf[n], z);
      const f32x4 lv = mfma32(pf, v4u{ONES, ONES, ONES, ONES}, z);
      LAS unsigned char* op = L + L_ATT + w * 2048 + kg * 512 + i16 * 32;
      *(LAS f32x4*)op = f32x4{o[0][0], o[1][1], o[2][2], o[3][3]};
      *(LAS f32x4*)(op + 16) = f32x4{o[4][0], o[5][1], o[6][2], o[7][3]};
      if (i16 == 4 * kg) *(LAS f32x2*)(L + L_ML + kg * 32 + w * 8) = f32x2{m, lv[0]};
    }
    LPOST(1 + layer);
    const unsigned tm = rtc();
    {
      f32x4 ml0, ml1; f32x2 ov[4];
      for (unsigned sp = 0; sp < (1u << 24); ++sp) {
        const v4u f = FL4(1 + layer);
        ml0 = *(volatile LAS f32x4*)(L + L_ML + w * 32); ml1 = *(volatile LAS f32x4*)(L + L_ML + w * 32 + 16);
#pragma unroll
        for (int k = 0; k < 4; ++k) ov[k] = *(volatile LAS f32x2*)(L + L_ATT + k * 2048 + w * 512 + lane * 8);
        if (FL4OK(f)) break;
      }
      const float M = vmax(vmax(ml0[0], ml0[2]), vmax(ml1[0], ml1[2]));
      const float e0 = __builtin_amdgcn_exp2f(ml0[0] - M), e1 = __builtin_amdgcn_exp2f(ml0[2] - M), e2 = __builtin_amdgcn_exp2f(ml1[0] - M), e3 = __builtin_amdgcn_exp2f(ml1[2] - M);
      const f32x2 a = (ov[0] * e0 + ov[1] * e1) + (ov[2] * e2 + ov[3] * e3);
      const float ls = fmaf(e0, ml0[1], e1 * ml0[3]) + fmaf(e2, ml1[1], e3 * ml1[3]);
      unsigned dv = pk(a[0], a[1]);
      unsigned d1 = dppu<0x130>(dv);
      unsigned d2 = dppu<0x130>(d1);
      dv = head3 ? dv : __float_as_uint(M); d1 = head3 ? d1 : __float_as_uint(ls); d2 = head3 ? d2 : tm;
      st_sc1(rs, ooff, v4u{dv, d1, d2, T});
      ooff += ostep; asm volatile("" : "+v"(ooff));
    }
    if (layer == 0) { STAMP(3);
      if (DN_LATE) {
#pragma unroll
        for (int n = 0; n < 2; ++n) *(LAS v4u*)(L + L_IDN + (2 * w + n) * 1024 + lane * 16) = Sw[8 + n];
      }
      LPOST(15);
    } else STAMP(15);
  }
}

#define RETRY(pi, reload) if (LIKELY(__all(ok)) || spins > SPIN_CAP) break; asm volatile("" ::: "memory"); DELAY(8); PASS(pi); if (!ok) { reload; }
#define SLIP(i, lat) do { const int late_ = (int)(rtc() - (X.tref + (unsigned)DLV(i) + (unsigned)(lat))); if (late_ > 0) X.tref += (unsigned)(late_ + DLV(15)); } while (0)
#define RESYNC(tmax, i, nom) do { const int late_ = (int)((tmax) - (X.tref + (unsigned)DLV(i) + (unsigned)(nom))); if (late_ > 0) X.tref += (unsigned)late_; } while (0)
#define QUAD_TMAX(tv) ({ int dt_ = (int)((tv) - X.tref); dt_ = max(dt_, (int)dppu<XOR1>((unsigned)dt_)); dt_ = max(dt_, (int)dppu<XOR2>((unsigned)dt_)); X.tref + (unsigned)__builtin_amdgcn_readfirstlane(dt_); })
#define LN_STATS(ds) float s1 = __uint_as_float((ds).x), s2 = __uint_as_float((ds).y); \
      s1 += dpp<XOR1>(s1); s2 += dpp<XOR1>(s2); s1 += dpp<XOR2>(s1); s2 += dpp<XOR2>(s2); \
      const float mean = s1 * (1.f / DM), rstd = __builtin_amdgcn_rsqf(fmaxf(fmaf(-mean, mean, s2 * (1.f / DM)), 0.f) + 1e-5f)

template <int layer>
__device__ __forceinline__ void run_B(Ctx& X, const v2u (&Bq)[8], v4u (&Aw)[12], v2u (&Bg)[8], v4u (&Bd)[7], float& xr, f32x4& csq, float (&csg)[2]) {
  const int lane = X.lane, w = X.w, kg = X.kg, i16 = X.i16, g = X.g, c = X.c, kq = c >> 3;
  const unsigned T = X.T; const __amdgpu_buffer_rsrc_t rs = X.rs; LAS unsigned char* L = X.L;
  LAS unsigned char* red = L + L_RED + layer * RED_SZ;
  const f32x4 z = {0.f, 0.f, 0.f, 0.f};
  const int grp = WS_GRP + g * GRP_SZ;
  v2u fy0; v4u c0, c1; unsigned tq;
  const int qun = kg * 8 + (lane & 7), qidx = ((lane >> 3) & 1) * 2048 + (32 * w + qun) * 16;
  const int xun = 8 * w + (lane & 7);
  {
    int qoff = (layer == 0 ? WS_Q0 + g * 8192 : grp + GO_Q1) + qidx, soff = grp + GO_ST2 + (lane & 3) * 16;
    PIN(qoff); PIN(soff);
    if (layer == 0) WAIT_AT(X.t0, 11); else WAIT_AT(X.tref, 4);
    v4u ds = v4u{0u, 0u, 0u, T}, r0, r1;
#define QLOADS() do { if (layer == 1) ds = ld_sc1(rs, soff); r0 = ld_sc1(rs, qoff); r1 = ld_sc1(rs, qoff + 4096); } while (0)
    QLOADS();
    SCHED();
    for (unsigned spins = 0;; ++spins) {
      LN_STATS(ds);
      const v4u d0 = unF(r0, T), d1 = unF(r1, T);
      float qv[4];
#pragma unroll
      for (int k = 0; k < 4; ++k) {
        float t = __uint_as_float(d0[k]) + __uint_as_float(d1[k]); t += dpp<ROR8>(t);
        qv[k] = layer == 0 ? t : rstd * fmaf(-mean, csq[k], t);
      }
      *(LAS v2u*)(L + L_QB + w * QSTR + qun * 8) = v2u{pk(qv[0], qv[1]), pk(qv[2], qv[3])};
      const bool ok = (((or4(d0) | or4(d1)) & 0xffu) == 0u) & (ds.w == T);
      RETRY(layer ? 7 : 0, QLOADS());
    }
    tq = ds.z;
    STAMP(2 + 12 * layer);
  }
  BARRIER();
  if (layer == 1) { RESYNC(QUAD_TMAX(tq), 3, NOM_Q); SLIP(4, 72); }
  if (layer == 0) {
    tq = rtc();
    if (w == TW && lane == 0) st2_sc1(rs, WS_FO + (g * 32 + c) * 128, v2u{T, tq});
    fy0 = ld2_sc1(rs, w == TW ? WS_FO + (g * 32 + (lane & 31)) * 128 : OOB);
    c0 = ld_sc1(rs, WS_Q0 + 65536 + g * 8192 + qidx); c1 = ld_sc1(rs, WS_Q0 + 65536 + g * 8192 + qidx + 4096);
    SCHED();
  }
#define WO_PREP() do { const int hh = w >> 1; \
_Pragma("unroll") \
    for (int t = 0; t < 6; ++t) { \
      const int un = 4 * t + kg, r0 = 6 * un + (i16 >> 2); \
      const int ra = r0 > 127 ? 127 : r0, rb = r0 + 4 > 127 ? 127 : r0 + 4; \
_Pragma("unroll") \
      for (int ct = 0; ct < 2; ++ct) { \
        const v2u lo = trread(L + L_IWO + (hh * 128 + ra) * 64 + ct * 32 + (i16 & 3) * 8), hi = trread(L + L_IWO + (hh * 128 + rb) * 64 + ct * 32 + (i16 & 3) * 8); \
        Aw[t * 2 + ct] = v4u{(t == 5 && un > 21) ? 0u : lo.x, (t == 5 && un > 20) ? 0u : lo.y, (t == 5 && un > 20) ? 0u : hi.x, 0u}; \
      } \
    } \
  } while (0)
  if (layer == 0) {
    WO_PREP();
#pragma unroll
    for (int u = 0; u < 8; ++u) Bg[u] = trread(L + L_IGU + (w >> 1) * 8192 + (64 * (u >> 2) + 16 * kg + 4 * (u & 3) + (i16 >> 2)) * 64 + (w & 1) * 32 + (i16 & 3) * 8);
    const v2u one = {ONES, ONES};
    const float cs = gemv128(one, one, Bg);
    const float p1 = dpp<0x55>(cs), p2 = dpp<0xAA>(cs), p3 = dpp<0xFF>(cs);
    st_sc1(rs, ((lane & 0x33) == 0 && (c & 7) < 7) ? WS_GC + g * 8192 + kq * 2048 + (w >> 1) * 1024 + (8 * (c & 7) + 4 * (w & 1) + (i16 >> 2)) * 16 : OOB, mkF(cs, p1, p2, p3, T));

  }
  {
    const int hh = w >> 1, st = w & 1, h = 2 * (c & 1) + hh;
    int obase = grp + GO_O + layer * O_L + h * 12288 + (16 * st + i16) * 16 + kg * 512;
    int o5 = obase + 5 * 2048 - (kg & 2) * 512;
    const int rot = (4 * kg + i16) & 15;
    int mlA = grp + GO_ML + layer * O_L + h * 512 + (16 * st + rot) * 16, mlB = mlA ^ 256;
    const int slot = ((lane >> 4) & 1) * 4 + (lane & 3), rep = ((lane >> 2) & 3) + 4 * (lane >> 5);
    int yst = WS_Y + layer * 262144 + rep * 32768 + (2 * g + (c & 1)) * 2048 + (c >> 1) * 128 + slot * 16;
    PIN(obase); PIN(o5); PIN(mlA); PIN(mlB); PIN(yst);
    if (layer == 0) {
      unsigned tg;
      if (w == TW) {
        for (unsigned spins = 0;; ++spins) {
          if ((lane & 31) == c) fy0 = v2u{T, tq};
          if (__all(fy0.x == T) || spins > SPIN_CAP) break;
          asm volatile("" ::: "memory"); DELAY(10); PASS(1);
          fy0 = ld2_sc1(rs, WS_FO + (g * 32 + (lane & 31)) * 128);
        }
        int dt = (int)(fy0.y - X.t0); dt = max(dt, (int)dppu<XOR1>((unsigned)dt)); dt = max(dt, (int)dppu<XOR2>((unsigned)dt)); dt = max(dt, (int)dppu<HMIR>((unsigned)dt));
        dt = max(dt, (int)dppu<MIR>((unsigned)dt)); dt = max(__builtin_amdgcn_readlane(dt, 0), __builtin_amdgcn_readlane(dt, 16)); tg = X.t0 + (unsigned)dt;
        *(volatile LAS unsigned*)(L + L_FL + 3 * 16) = tg; LPOST(3);
      } else { LWAIT1(3, TW); tg = __builtin_amdgcn_readfirstlane(*(volatile LAS unsigned*)(L + L_FL + 3 * 16)); }
      if (c == 0 && w == TW && lane < 8) st2_sc1(rs, WS_FY + (lane * 8 + g) * 128, v2u{T, tg});
      for (unsigned spins = 0;; ++spins) {
        c0 = unF(c0, T); c1 = unF(c1, T);
        if (__all(((or4(c0) | or4(c1)) & 0xffu) == 0u) || spins > SPIN_CAP) break;
        asm volatile("" ::: "memory"); DELAY(8); c0 = ld_sc1(rs, WS_Q0 + 65536 + g * 8192 + qidx); c1 = ld_sc1(rs, WS_Q0 + 65536 + g * 8192 + qidx + 4096);
      }
#pragma unroll
      for (int k = 0; k < 4; ++k) { float t = __uint_as_float(c0[k]) + __uint_as_float(c1[k]); t += dpp<ROR8>(t); csq[k] = t; }
      { unsigned t_ = tg + (unsigned)DLV(0); for (int k_ = 0; k_ < 4096 && (int)(rtc() - t_) < 0; ++k_) {} }
      X.tref = tg;
    } else WAIT_AT(X.tref, 5);
    if (layer == 0) fy0 = ld2_sc1(rs, WS_FY + (g * 8 + (lane & 7)) * 128);
    v4u d[8];
#define OLOADS() do { d[6] = ld_sc1(rs, mlA); d[7] = ld_sc1(rs, mlB); _Pragma("unroll") for (int t = 0; t < 5; ++t) d[t] = ld_sc1(rs, obase + t * 2048); d[5] = ld_sc1(rs, o5); } while (0)
    OLOADS();
    SCHED();
    for (unsigned spins = 0;; ++spins) {
      const float mA = __uint_as_float(d[6].x), lA = __uint_as_float(d[6].y), mB = __uint_as_float(d[7].x), lB = __uint_as_float(d[7].y);
      const float M = row_max(vmax(mA, mB));
      const float eA = __builtin_amdgcn_exp2f(mA - M), eB = __builtin_amdgcn_exp2f(mB - M);
      const float Ls = row_sum(fmaf(eA, lA, eB * lB));
      const float wgt = eA * __builtin_amdgcn_rcpf(Ls);
      const float w0 = dpp<0x150>(wgt), w1 = dpp<0x151>(wgt), w2 = dpp<0x152>(wgt), w3 = dpp<0x153>(wgt);
      f32x4 a0 = z, a1 = z;
#pragma unroll
      for (int t = 0; t < 6; ++t) { a0 = mfma32(d[t], Aw[2 * t], a0); a1 = mfma32(d[t], Aw[2 * t + 1], a1); }
      const float y0 = fmaf(a0[0], w0, a0[1] * w1) + fmaf(a0[2], w2, a0[3] * w3), y1 = fmaf(a1[0], w0, a1[1] * w1) + fmaf(a1[2], w2, a1[3] * w3);
      auto sw = __builtin_amdgcn_permlane16_swap(__float_as_uint(y0), __float_as_uint(y1), false, false);
      const float yy = __uint_as_float(sw[0]) + __uint_as_float(sw[1]);
      auto sx = __builtin_amdgcn_permlane32_swap(__float_as_uint(yy), __float_as_uint(yy), false, false);
      *(LAS float*)(red + w * RED_ROW + (lane & 31) * 4) = __uint_as_float(sx[0]) + __uint_as_float(sx[1]);
      const bool ok = (d[0].w == T) & (d[1].w == T) & (d[2].w == T) & (d[3].w == T) & (d[4].w == T) & (d[5].w == T) & (d[6].w == T) & (d[7].w == T);
      RETRY(layer ? 8 : 1, OLOADS());
    }
    if (layer == 0) asm volatile("s_waitcnt vmcnt(0)" ::: "memory");
    LPOST(4 + 5 * layer);
    STAMP(4 + 12 * layer);
    if (w == TW) {
      f32x4 v;
      for (unsigned sp = 0; sp < (1u << 24); ++sp) {
        const v4u f = FL4(4 + 5 * layer);
        v = *(volatile LAS f32x4*)(red + ((lane >> 2) & 3) * RED_ROW + slot * 16);
        if (FL4OK(f)) break;
      }
#pragma unroll
      for (int k = 0; k < 4; ++k) { float t = v[k]; t += dpp<ROR8>(t); t += dpp<ROR4>(t); v[k] = t; }
      st_sc1(rs, yst, mkF(v[0], v[1], v[2], v[3], T));
    }
    if (layer == 1) {
      int dt = max((int)(d[6].z - X.tref), (int)(d[7].z - X.tref));
      dt = max(dt, (int)dppu<XOR1>((unsigned)dt)); dt = max(dt, (int)dppu<XOR2>((unsigned)dt)); dt = max(dt, (int)dppu<HMIR>((unsigned)dt)); dt = max(dt, (int)dppu<MIR>((unsigned)dt));
      RESYNC(X.tref + (unsigned)__builtin_amdgcn_readfirstlane(dt), 4, NOM_O);
      SLIP(5, 125);
    }
  }
  STAMP(5 + 12 * layer);
#define X_SHARE(xb, setx, sets) do { LPOST(setx); \
    float s1 = xr, s2 = xr * xr; \
    s1 += dpp<XOR1>(s1); s2 += dpp<XOR1>(s2); s1 += dpp<XOR2>(s1); s2 += dpp<XOR2>(s2); s1 += dpp<HMIR>(s1); s2 += dpp<HMIR>(s2); \
    *(LAS f32x2*)((xb) + 256 + (4 * w + kg) * 8) = f32x2{s1, s2}; LPOST(sets); } while (0)
#define X_FETCH(xb, setx) v2u a0, a1; for (unsigned sp = 0; sp < (1u << 24); ++sp) { const v4u f = FL4(setx); \
      a0 = *(volatile LAS v2u*)((xb) + (4 * kg + (i16 & 3)) * 8); a1 = *(volatile LAS v2u*)((xb) + 128 + (4 * kg + (i16 & 3)) * 8); if (FL4OK(f)) break; }
#define STATS_PUBLISH(xb, sets, idx) do { f32x2 sp2; for (unsigned sp = 0; sp < (1u << 24); ++sp) { const v4u f = FL4(sets); sp2 = *(volatile LAS f32x2*)((xb) + 256 + i16 * 8); if (FL4OK(f)) break; } \
    const float S1 = row_sum(sp2[0]), S2 = row_sum(sp2[1]); \
    st_sc1(rs, lane == 0 ? grp + (idx) + kq * 16 : OOB, v4u{__float_as_uint(S1), __float_as_uint(S2), rtc(), T}); } while (0)
  {
    const int go = kq * 2048 + (w >> 1) * 1024 + (8 * (c & 7) + 4 * (w & 1) + (i16 >> 2)) * 16;
    const bool gst = (lane & 0x33) == 0 && (c & 7) < 7;
    int goff = gst ? grp + GO_G + layer * O_L + go : OOB;
    int yoff = WS_Y + layer * 262144 + g * 32768 + (lane >> 3) * 2048 + (32 * kq + xun) * 16;
    PIN(goff); PIN(yoff);
    if (layer == 0) {
      {
        for (unsigned spins = 0;; ++spins) {
          if ((lane & 7) == g) fy0 = v2u{T, X.tref};
          if (__all(fy0.x == T) || spins > SPIN_CAP) break;
          asm volatile("" ::: "memory"); DELAY(14); PASS(3);
          fy0 = ld2_sc1(rs, WS_FY + (g * 8 + (lane & 7)) * 128);
        }
        int dt = (int)(fy0.y - X.t0); dt = max(dt, (int)dppu<XOR1>((unsigned)dt)); dt = max(dt, (int)dppu<XOR2>((unsigned)dt)); dt = max(dt, (int)dppu<HMIR>((unsigned)dt));
        X.tref = X.t0 + (unsigned)__builtin_amdgcn_readfirstlane(dt);
      }
      { unsigned t_ = X.tref + (unsigned)DLV(1); for (int k_ = 0; k_ < 4096 && (int)(rtc() - t_) < 0; ++k_) {} }
    } else WAIT_AT(X.tref, 6);
    LAS unsigned char* xb = L + L_XB + 2 * layer * XB_SZ;
    float xn;
    v4u r0 = ld_sc1(rs, yoff), r1 = ld_sc1(rs, yoff + 16384);
    SCHED();
    for (unsigned spins = 0;; ++spins) {
      const v4u d0 = unF(r0, T), d1 = unF(r1, T);
      float t = rows_reduce4(__builtin_bit_cast(f32x4, d0) + __builtin_bit_cast(f32x4, d1));
      t += dpp<ROR8>(t);
      const unsigned p = pk(xr + t, 0.f);
      *(LAS u16*)(xb + (4 * xun + kg) * 2) = (u16)p;
      xn = bflo(p);
      const bool ok = ((or4(d0) | or4(d1)) & 0xffu) == 0u;
      RETRY(layer ? 9 : 4, r0 = ld_sc1(rs, yoff); r1 = ld_sc1(rs, yoff + 16384));
    }
    xr = xn;
    STAMP(6 + 12 * layer);
    X_SHARE(xb, 6 + 4 * layer, 12 + layer);
    if ((c & 7) < 7) {
      X_FETCH(xb, 6 + 4 * layer);
      const float part = gemv128(a0, a1, Bg);
      const float p1 = dpp<0x55>(part), p2 = dpp<0xAA>(part), p3 = dpp<0xFF>(part);
      st_sc1(rs, goff, mkF(part, p1, p2, p3, T));
    } else if (w == 0) STATS_PUBLISH(xb, 12 + layer, GO_ST + layer * O_L);
  }
  if (layer == 1) STAMP(19);
  SLIP(layer == 0 ? 1 : 6, 98);
  if (layer == 0) {
    const v4u zz = {0u, 0u, 0u, 0u};
    const int slot = ((lane >> 4) & 1) * 4 + (lane & 3), rep = ((lane >> 2) & 3) + 4 * (lane >> 5);
    if (w == TW) {
      st_sc1(rs, WS_Y + 262144 + rep * 32768 + (2 * g + (c & 1)) * 2048 + (c >> 1) * 128 + slot * 16, zz);
      st_sc1(rs, i16 < 8 ? WS_X + 131072 + i16 * 16384 + g * 2048 + c * 64 + kg * 16 : OOB, zz);
    }
    st_sc1(rs, lane < 30 ? grp + O_L + (c * 4 + w) * 480 + lane * 16
                         : ((lane & 0x33) == 0x20 ? grp + GO_Q1 + kq * 2048 + ((c & 7) * 64 + w * 16 + i16) * 4 : ((lane == 63 && (c & 7) == 0 && w == 0) ? grp + GO_ST2 + kq * 16 : OOB)), zz);
  }
  {
    const int hu = 14 * w + (i16 < 14 ? i16 : 13);
    const int gi = kg * 2048 + hu * 16;
    int gld = grp + GO_G + layer * O_L + gi, soff = grp + GO_ST + layer * O_L + (lane & 3) * 16;
    int xst = i16 < 8 ? WS_X + layer * 131072 + i16 * 16384 + g * 2048 + c * 64 + kg * 16 : OOB;
    PIN(gld); PIN(soff); PIN(xst);
    if (layer == 0 && w == TW) {
      LWAIT4(15);
#pragma unroll
      for (int t = 0; t < 7; ++t) {
        const int r0 = 32 * t + 8 * kg + (i16 >> 2);
        const v2u lo = trread(L + L_IDN + r0 * 32 + (i16 & 3) * 8), hi = trread(L + L_IDN + (r0 + 4) * 32 + (i16 & 3) * 8);
        Bd[t] = v4u{lo.x, lo.y, hi.x, hi.y};
      }
    }
    WAIT_AT(X.tref, layer == 0 ? 2 : 7);
    LAS unsigned char* hb = L + L_HB + layer * 512;
    v4u ds, r0, r1;
#define GLOADS() do { ds = ld_sc1(rs, soff); if (layer == 0) { c0 = ld_sc1(rs, WS_GC + g * 8192 + gi); c1 = ld_sc1(rs, WS_GC + g * 8192 + gi + 1024); } \
      r0 = ld_sc1(rs, gld); r1 = ld_sc1(rs, gld + 1024); } while (0)
    GLOADS();
    SCHED();
    for (unsigned spins = 0;; ++spins) {
      LN_STATS(ds);
      unsigned cbad = 0u;
      if (layer == 0) {
        const v4u e0 = unF(c0, T), e1 = unF(c1, T); cbad = or4(e0) | or4(e1);
        csg[0] = rows_reduce4(__builtin_bit_cast(f32x4, e0)); csg[1] = rows_reduce4(__builtin_bit_cast(f32x4, e1));
      }
      const v4u d0 = unF(r0, T), d1 = unF(r1, T);
      const float gv = rstd * fmaf(-mean, csg[0], rows_reduce4(__builtin_bit_cast(f32x4, d0)));
      const float uv = rstd * fmaf(-mean, csg[1], rows_reduce4(__builtin_bit_cast(f32x4, d1)));
      const float hv = uv * __builtin_amdgcn_rcpf(1.f + __builtin_amdgcn_exp2f(-1.4426950408889634f * gv));
      *(LAS u16*)(hb + (4 * hu + kg) * 2) = (u16)pk(hv, 0.f);
      const bool ok = (((or4(d0) | or4(d1) | cbad) & 0xffu) == 0u) & (ds.w == T);
      RETRY(5, GLOADS());
    }
    if (layer == 0) asm volatile("s_waitcnt vmcnt(0)" ::: "memory");
    LPOST(7 + 4 * layer);
    STAMP(8 + 12 * layer);
    if (w == TW) {
      v4u hf[7];
      for (unsigned sp = 0; sp < (1u << 24); ++sp) {
        const v4u f = FL4(7 + 4 * layer);
#pragma unroll
        for (int t = 0; t < 7; ++t) hf[t] = *(volatile LAS v4u*)(hb + t * 64 + kg * 16);
        if (FL4OK(f)) break;
      }
      f32x4 a0 = z, a1 = z;
#pragma unroll
      for (int t = 0; t < 7; ++t) { if (t & 1) a1 = mfma32(Bd[t], hf[t], a1); else a0 = mfma32(Bd[t], hf[t], a0); }
      a0 += a1;
      st_sc1(rs, xst, mkF(a0[0], a0[1], a0[2], a0[3], T));
    }
    if (layer == 0) STAMP(10);
    RESYNC(QUAD_TMAX(ds.z), layer == 0 ? 1 : 6, NOM_G);
    SLIP(layer == 0 ? 2 : 7, layer == 0 ? 125 : 105);
  }
  if (layer == 0 || (g == 0 && (c & 7) == 0)) {
    int xoff = WS_X + layer * 131072 + g * 16384 + (lane >> 3) * 2048 + (32 * kq + xun) * 16;
    int qst = (lane & 0x33) == 0 ? grp + GO_Q1 + kq * 2048 + ((c & 7) * 64 + w * 16 + i16) * 4 : OOB;
    PIN(xoff); PIN(qst);
    WAIT_AT(X.tref, layer == 0 ? 3 : 9);
    LAS unsigned char* xb = L + L_XB + XB_SZ;
    unsigned p;
    v4u r0 = ld_sc1(rs, xoff);
    for (unsigned spins = 0;; ++spins) {
      const v4u d0 = unF(r0, T);
      float t = rows_reduce4(__builtin_bit_cast(f32x4, d0));
      t += dpp<ROR8>(t);
      p = pk(xr + t, 0.f);
      if (layer == 0) *(LAS u16*)(xb + (4 * xun + kg) * 2) = (u16)p;
      const bool ok = (or4(d0) & 0xffu) == 0u;
      RETRY(6, r0 = ld_sc1(rs, xoff));
    }
    if (layer == 0) {
      xr = bflo(p);
      X_SHARE(xb, 8, 14);
      X_FETCH(xb, 8);
      const float part = gemv128(a0, a1, Bq);
      const float p1 = dpp<0x55>(part), p2 = dpp<0xAA>(part), p3 = dpp<0xFF>(part);
      st_sc1(rs, qst, mkF(part, p1, p2, p3, T));
      if ((c & 7) == 0 && w == 0) STATS_PUBLISH(xb, 14, GO_ST2);
      STAMP(11);
      SLIP(3, 100);
    } else {
      if ((lane & 8) == 0) *(GAS u16*)(X.out + 128 * kq + 4 * xun + kg) = (u16)p;
      if (c == 0 && w == 0 && lane == 0) st1_sc1(rs, 0, X.E + 1u);
      STAMP(21);
    }
  }
}

__global__ void __launch_bounds__(512, 2)
fused_decode(const u16* x, const u16* kc, const u16* vc, const u16* wq, const u16* wo, const u16* wg, const u16* wu, const u16* wd, u16* out, unsigned char* ws) {
  extern __shared__ __attribute__((aligned(16))) unsigned char smem[];
  Ctx X;
  const int tid = threadIdx.x;
  X.lane = tid & 63; X.wv = __builtin_amdgcn_readfirstlane(tid >> 6); X.w = X.wv & 3; X.kg = X.lane >> 4; X.i16 = X.lane & 15;
  X.b = blockIdx.x; X.g = X.b & 7; X.c = X.b >> 3;
  X.t0 = rtc();
  unsigned pfv;
  {
    unsigned long long pc; unsigned len;
    asm volatile("s_getpc_b64 %0\n\tLpf_here:\n\ts_mov_b32 %1, Lpf_end - Lpf_here" : "=s"(pc), "=s"(len));
    unsigned off = (unsigned)(X.c * 8 + X.wv) * 128u; off = off < len ? off : 0u;
    pfv = *(const GAS unsigned*)(pc + off);
  }
  SCHED();
  asm volatile("" : "+s"(x), "+s"(kc), "+s"(vc), "+s"(wq), "+s"(wo), "+s"(wg), "+s"(wu), "+s"(wd), "+s"(out), "+s"(ws));
  SCHED();
  X.vc = vc; X.wo = wo; X.wg = wg; X.wu = wu; X.wd = wd; X.out = out; X.ws = ws;
  X.L = (LAS unsigned char*)smem;
  X.rs = __builtin_amdgcn_make_buffer_rsrc((void*)ws, 0, WS_BYTES, 0x00020000);
  const int lane = X.lane, w = X.w, kg = X.kg, i16 = X.i16, g = X.g, c = X.c, kq = c >> 3;
  const __amdgpu_buffer_rsrc_t rs = X.rs; LAS unsigned char* L = X.L;
  v4u Kf[8], Vf[8], Sw[10], wr[4], xf; v2u xa, xb; unsigned xu, E;
  if (X.wv >= 4) {
    DELAY(12);
    const size_t kvbase = ((size_t)g * SK + (SK - WIN) + 128 * c + 32 * w) * DH;
#pragma unroll
    for (int s = 0; s < 8; ++s) Kf[s] = ldg_nt(kc + kvbase + s * 512 + lane * 8);
#pragma unroll
    for (int j = 0; j < VPRE; ++j) Vf[j] = ldg_nt(vc + kvbase + (16 * (j >> 2) + 4 * kg + (j & 3)) * DH + 8 * i16);
  } else {
#pragma unroll
    for (int n = 0; n < 4; ++n) wr[n] = ldg_nt(wq + (size_t)(128 * kq + 32 * w + 8 * n + (lane >> 3)) * 4096 + 512 * g + 64 * (c & 7) + 8 * (lane & 7));
    xf = *(const GAS v4u*)(x + 8 * lane);
    xa = *(const GAS v2u*)(x + 4 * (32 * kq + 4 * kg + (i16 & 3))); xb = *(const GAS v2u*)(x + 4 * (32 * kq + 16 + 4 * kg + (i16 & 3)));
    xu = *(const GAS u16*)(x + 128 * kq + 4 * (8 * w + (lane & 7)) + kg);
    E = ld1_sc1(rs, 0);
    SCHED();
    {
      const v4u zz = {0u, 0u, 0u, 0u};
      const int slot = ((lane >> 4) & 1) * 4 + (lane & 3), rep = ((lane >> 2) & 3) + 4 * (lane >> 5), grp = WS_GRP + g * GRP_SZ;
      if (w == TW) {
        st_sc1(rs, WS_Y + rep * 32768 + (2 * g + (c & 1)) * 2048 + (c >> 1) * 128 + slot * 16, zz);
        st_sc1(rs, i16 < 8 ? WS_X + i16 * 16384 + g * 2048 + c * 64 + kg * 16 : OOB, zz);
      }
      st_sc1(rs, (c & 7) < 7 ? ((lane & 0x33) == 0 ? grp + GO_G + kq * 2048 + (w >> 1) * 1024 + (8 * (c & 7) + 4 * (w & 1) + (i16 >> 2)) * 16 : OOB)
                             : ((w == 0 && lane == 0) ? grp + GO_ST + kq * 16 : OOB), zz);
    }
  }
  SCHED();
  int wv2 = X.wv; asm volatile("" : "+s"(wv2));
  if (wv2 >= 4) run_A(X, Kf, Vf, Sw);
  int wv3 = X.wv; asm volatile("" : "+s"(wv3));
  if (wv3 < 4) {
#pragma unroll
    for (int n = 0; n < 4; ++n) *(LAS v4u*)(L + L_IMG + w * 4096 + n * 1024 + lane * 16) = wr[n];
    const unsigned hsh = (E + 1u) * 0x9E3779B1u;
    const unsigned T = __builtin_amdgcn_readfirstlane(((hsh & 0xbfffbf00u) | (((hsh >> 8) & 0xbfu) ^ 0xa5u)));
    X.E = E; X.T = T;
    LPOST(0);
    float xr = bflo(xu);
    v2u Bq[8], Bg[8]; v4u Aw[12], Bd[7];
    {
      float xv[8] = {bflo(xf.x), bfhi(xf.x), bflo(xf.y), bfhi(xf.y), bflo(xf.z), bfhi(xf.z), bflo(xf.w), bfhi(xf.w)};
      float s = 0.f;
#pragma unroll
      for (int k = 0; k < 8; ++k) s += xv[k];
      const float mean = xrow_sum(row_sum(s)) * (1.f / DM);
      float v = 0.f;
#pragma unroll
      for (int k = 0; k < 8; ++k) { const float d = xv[k] - mean; v = fmaf(d, d, v); }
      const float var = xrow_sum(row_sum(v)) * (1.f / DM);
      const float mb = rbf(mean), isd = 1.0f / rbf(sqrtf(rbf(rbf(var) + 1e-5f)));
      const float xi[8] = {bflo(xa.x), bfhi(xa.x), bflo(xa.y), bfhi(xa.y), bflo(xb.x), bfhi(xb.x), bflo(xb.y), bfhi(xb.y)};
      float xn[8];
#pragma unroll
      for (int k = 0; k < 8; ++k) xn[k] = rbf(xi[k] - mb) * isd;
      const v2u a0 = {pk(xn[0], xn[1]), pk(xn[2], xn[3])}, a1 = {pk(xn[4], xn[5]), pk(xn[6], xn[7])};
      LWAIT4(0);
#pragma unroll
      for (int u = 0; u < 8; ++u) Bq[u] = trread(L + L_IMG + (64 * (u >> 2) + 16 * kg + 4 * (u & 3) + (i16 >> 2)) * 128 + w * 32 + (i16 & 3) * 8);
      const int qo = (lane & 0x33) == 0 ? WS_Q0 + g * 8192 + kq * 2048 + ((c & 7) * 64 + w * 16 + i16) * 4 : OOB - 65536;
      {
        const float part = gemv128(a0, a1, Bq);
        const float p1 = dpp<0x55>(part), p2 = dpp<0xAA>(part), p3 = dpp<0xFF>(part);
        st_sc1(rs, qo, mkF(part, p1, p2, p3, T));
      }
      STAMP(1);
      {
        const v2u one = {ONES, ONES};
        const float part = gemv128(one, one, Bq);
        const float p1 = dpp<0x55>(part), p2 = dpp<0xAA>(part), p3 = dpp<0xFF>(part);
        st_sc1(rs, qo + 65536, mkF(part, p1, p2, p3, T));
      }
    }
    f32x4 csq = {0.f, 0.f, 0.f, 0.f}; float csg[2] = {0.f, 0.f};
    run_B<0>(X, Bq, Aw, Bg, Bd, xr, csq, csg);
    run_B<1>(X, Bq, Aw, Bg, Bd, xr, csq, csg);
  }
  asm volatile("" :: "v"(pfv));
  STAMP_FLUSH();
  asm volatile("Lpf_end:");
}

extern "C" void kernel_launch(void* const* d_in, const int* in_sizes, int n_in, void* d_out, int out_size, void* d_ws, size_t ws_size,
                              hipStream_t stream) {
  (void)in_sizes; (void)n_in; (void)out_size; (void)ws_size;
  (void)hipFuncSetAttribute((const void*)fused_decode, hipFuncAttributeMaxDynamicSharedMemorySize, LDS_BYTES);
  hipLaunchKernelGGL(fused_decode, dim3(256), dim3(512), LDS_BYTES, stream, (const u16*)d_in[0], (const u16*)d_in[1], (const u16*)d_in[2],
                     (const u16*)d_in[3], (const u16*)d_in[4], (const u16*)d_in[6], (const u16*)d_in[7], (const u16*)d_in[8], (u16*)d_out,
                     (unsigned char*)d_ws);
}
```

```cpp
#define DIAG 0
#include <hip/hip_runtime.h>
#include <stdint.h>

typedef unsigned short u16;
typedef unsigned v4u __attribute__((ext_vector_type(4)));
typedef unsigned v2u __attribute__((ext_vector_type(2)));
typedef float f32x4 __attribute__((ext_vector_type(4)));
typedef float f32x2 __attribute__((ext_vector_type(2)));
typedef short s16x4 __attribute__((ext_vector_type(4)));
typedef __bf16 bf16x8 __attribute__((ext_vector_type(8)));
typedef __bf16 bf16x2 __attribute__((ext_vector_type(2)));
#define LAS __attribute__((address_space(3)))
#define GAS __attribute__((address_space(1)))

constexpr int SK = 32768, WIN = 4096, DH = 128, DM = 512, FF = 1792;
constexpr int WS_FO = 4096;
constexpr int WS_FY = WS_FO + 32768;
constexpr int WS_X = WS_FY + 8192;
constexpr int WS_Y = WS_X + 262144;
constexpr int WS_Q0 = WS_Y + 524288;
constexpr int WS_GC = WS_Q0 + 131072;
constexpr int WS_GRP = WS_GC + 65536, GRP_SZ = 143360, O_L = 61440;
constexpr int GO_ML = 0;
constexpr int GO_O = 2048;
constexpr int GO_G = 51200;
constexpr int GO_ST = 59392;
constexpr int GO_ST2 = O_L + 61440 + 8192;
constexpr int GO_Q1 = O_L + 61440;
static_assert(GO_ST2 + 128 <= GRP_SZ && 128 * 480 == O_L, "group block");
constexpr int WS_BYTES = WS_GRP + 8 * GRP_SZ;
constexpr int OOB = 0x7fffff00;
constexpr int WS_STAMP = 8 << 20;
constexpr int L_IMG = 0;
constexpr int L_IGU = 16384;
constexpr int L_IDN = 32768;
constexpr int L_IWO = 40960;
constexpr int L_STG = 57344, STG_W = 8704;
constexpr int L_ATT = L_STG + 4 * STG_W;
constexpr int L_ML = L_ATT + 8192;
constexpr int L_QB = L_ML + 128, QSTR = 320;
constexpr int L_XB = L_QB + 4 * QSTR;
constexpr int XB_SZ = 384;
constexpr int L_HB = L_XB + 3 * XB_SZ;
constexpr int L_FL = L_HB + 1024;
constexpr int L_RED = L_FL + 256, RED_ROW = 144, RED_SZ = 4 * RED_ROW;
constexpr int LDS_BYTES = 112 * 1024;
static_assert(L_RED + 2 * RED_SZ <= LDS_BYTES, "lds");
constexpr int TW = 3;
constexpr unsigned SPIN_CAP = 1u << 17;

constexpr int DL[16] = {  105,   290,   420,   555,   675,   825,   970,   1090,
                          3,   1220,   1,   400,   20,
                          15,   4,   40};

constexpr int NOM_G = 85, NOM_Q = 92, NOM_O = 120;
#define SCHED() __builtin_amdgcn_sched_barrier(0)
#define STAMP(i) do {} while (0)
#define PASS(i) do {} while (0)
#define STAMP_FLUSH() do {} while (0)
#define DELAY(i) do { if (DL[i] > 0) __builtin_amdgcn_s_sleep(DL[i]); } while (0)
#define DLV(i) DL[i]
__device__ __forceinline__ unsigned rtc() { return (unsigned)__builtin_amdgcn_s_memrealtime(); }
#define WAIT_AT(ref, i) do { SCHED(); unsigned tg_ = (ref) + (unsigned)DLV(i); const unsigned lo_ = rtc() + (unsigned)DLV(13); if ((int)(lo_ - tg_) > 0) tg_ = lo_; \
    for (int k_ = 0; k_ < 4096 && (int)(rtc() - tg_) < 0; ++k_) {} SCHED(); } while (0)
#define PW_A 0
#define VPRE 0
#define DN_LATE 1
#define PW_B 1
#define UNLIKELY(x) __builtin_expect(!!(x), 0)
#define LIKELY(x) __builtin_expect(!!(x), 1)
#define PIN(v) asm volatile("" : "+v"(v))

__device__ __forceinline__ float bflo(unsigned u) { return __uint_as_float(u << 16); }
__device__ __forceinline__ float bfhi(unsigned u) { return __uint_as_float(u & 0xffff0000u); }
__device__ __forceinline__ unsigned pk(float lo, float hi) { bf16x2 p = {(__bf16)lo, (__bf16)hi}; return __builtin_bit_cast(unsigned, p); }
__device__ __forceinline__ float rbf(float f) { return bflo((unsigned)__builtin_bit_cast(u16, (__bf16)f)); }
__device__ __forceinline__ float vmax(float a, float b) { return __builtin_elementwise_maximum(a, b); }
template <int CTRL> __device__ __forceinline__ float dpp(float x) {
  return __builtin_bit_cast(float, __builtin_amdgcn_mov_dpp(__builtin_bit_cast(int, x), CTRL, 0xf, 0xf, true));
}
template <int CTRL> __device__ __forceinline__ unsigned dppu(unsigned x) { return (unsigned)__builtin_amdgcn_mov_dpp((int)x, CTRL, 0xf, 0xf, true); }
constexpr int XOR1 = 0xB1, XOR2 = 0x4E, HMIR = 0x141, MIR = 0x140, ROR4 = 0x124, ROR8 = 0x128;
__device__ __forceinline__ float row_sum(float v) { v += dpp<XOR1>(v); v += dpp<XOR2>(v); v += dpp<HMIR>(v); v += dpp<MIR>(v); return v; }
__device__ __forceinline__ float row_max(float v) { v = vmax(v, dpp<XOR1>(v)); v = vmax(v, dpp<XOR2>(v)); v = vmax(v, dpp<HMIR>(v)); v = vmax(v, dpp<MIR>(v)); return v; }
__device__ __forceinline__ float xrow_sum(float x) {
  auto s = __builtin_amdgcn_permlane16_swap(__float_as_uint(x), __float_as_uint(x), false, false);
  x = __uint_as_float(s[0]) + __uint_as_float(s[1]);
  auto t = __builtin_amdgcn_permlane32_swap(__float_as_uint(x), __float_as_uint(x), false, false);
  return __uint_as_float(t[0]) + __uint_as_float(t[1]);
}
__device__ __forceinline__ float xrow_max(float x) {
  auto s = __builtin_amdgcn_permlane16_swap(__float_as_uint(x), __float_as_uint(x), false, false);
  x = vmax(__uint_as_float(s[0]), __uint_as_float(s[1]));
  auto t = __builtin_amdgcn_permlane32_swap(__float_as_uint(x), __float_as_uint(x), false, false);
  return vmax(__uint_as_float(t[0]), __uint_as_float(t[1]));
}
__device__ __forceinline__ float rows_reduce4(f32x4 v) {
  auto a = __builtin_amdgcn_permlane16_swap(__float_as_uint(v[0]), __float_as_uint(v[1]), false, false);
  auto b = __builtin_amdgcn_permlane16_swap(__float_as_uint(v[2]), __float_as_uint(v[3]), false, false);
  const float t01 = __uint_as_float(a[0]) + __uint_as_float(a[1]), t23 = __uint_as_float(b[0]) + __uint_as_float(b[1]);
  auto c = __builtin_amdgcn_permlane32_swap(__float_as_uint(t01), __float_as_uint(t23), false, false);
  return __uint_as_float(c[0]) + __uint_as_float(c[1]);
}
__device__ __forceinline__ f32x4 mfma32(v4u a, v4u b, f32x4 c) {
  return __builtin_amdgcn_mfma_f32_16x16x32_bf16(__builtin_bit_cast(bf16x8, a), __builtin_bit_cast(bf16x8, b), c, 0, 0, 0);
}
__device__ __forceinline__ f32x4 mfma16(v2u a, v2u b, f32x4 c) {
  return __builtin_amdgcn_mfma_f32_16x16x16bf16_1k(__builtin_bit_cast(s16x4, a), __builtin_bit_cast(s16x4, b), c, 0, 0, 0);
}
__device__ __forceinline__ float dot2(unsigned a, unsigned b, float c) {
  return __builtin_amdgcn_fdot2_f32_bf16(__builtin_bit_cast(bf16x2, a), __builtin_bit_cast(bf16x2, b), c, false);
}
__device__ __forceinline__ v2u trread(LAS unsigned char* p) { return __builtin_bit_cast(v2u, __builtin_amdgcn_ds_read_tr16_b64_v4i16((LAS s16x4*)p)); }
__device__ __forceinline__ v4u ldg_nt(const u16* p) { return __builtin_nontemporal_load((const GAS v4u*)p); }
__device__ __forceinline__ v4u ld_sc1(__amdgpu_buffer_rsrc_t rs, int off) { return __builtin_amdgcn_raw_buffer_load_b128(rs, off, 0, 16); }
__device__ __forceinline__ unsigned ld1_sc1(__amdgpu_buffer_rsrc_t rs, int off) { return __builtin_amdgcn_raw_buffer_load_b32(rs, off, 0, 16); }
__device__ __forceinline__ v2u ld2_sc1(__amdgpu_buffer_rsrc_t rs, int off) { return __builtin_amdgcn_raw_buffer_load_b64(rs, off, 0, 16); }
__device__ __forceinline__ void st2_sc1(__amdgpu_buffer_rsrc_t rs, int off, v2u v) { __builtin_amdgcn_raw_buffer_store_b64(v, rs, off, 0, 16); }
__device__ __forceinline__ void st_sc1(__amdgpu_buffer_rsrc_t rs, int off, v4u v) { __builtin_amdgcn_raw_buffer_store_b128(v, rs, off, 0, 16); }
__device__ __forceinline__ void st1_sc1(__amdgpu_buffer_rsrc_t rs, int off, unsigned v) { __builtin_amdgcn_raw_buffer_store_b32(v, rs, off, 0, 16); }
#define PERM(hi, lo, sel) __builtin_amdgcn_perm((hi), (lo), (sel))
__device__ __forceinline__ bool okH(v4u d, unsigned T) { return (PERM(d.y, d.x, 0x05040100u) == T) & (PERM(d.w, d.z, 0x05040100u) == T); }
__device__ __forceinline__ v2u valH(v4u d) { return v2u{PERM(d.y, d.x, 0x07060302u), PERM(d.w, d.z, 0x07060302u)}; }
__device__ __forceinline__ v4u mkH(unsigned p0, unsigned p1, unsigned T) {
  return v4u{PERM(p0, T, 0x05040100u), PERM(p0, T, 0x07060302u), PERM(p1, T, 0x05040100u), PERM(p1, T, 0x07060302u)};
}
__device__ __forceinline__ v4u unF(v4u d, unsigned T) { return v4u{d.x ^ (T & 0xffu), d.y ^ ((T >> 8) & 0xffu), d.z ^ ((T >> 16) & 0xffu), d.w ^ (T >> 24)}; }
__device__ __forceinline__ unsigned or4(v4u d) { return (d.x | d.y) | (d.z | d.w); }
__device__ __forceinline__ v4u mkF(float a, float b, float c, float d, unsigned T) {
  return v4u{PERM(__float_as_uint(a), T, 0x07060500u), PERM(__float_as_uint(b), T, 0x07060501u),
             PERM(__float_as_uint(c), T, 0x07060502u), PERM(__float_as_uint(d), T, 0x07060503u)};
}
#define LGKM0() asm volatile("s_waitcnt lgkmcnt(0)" ::: "memory")
#define BARRIER() do { LGKM0(); __builtin_amdgcn_s_barrier(); asm volatile("" ::: "memory"); } while (0)

__device__ __forceinline__ float gemv128(v2u a0, v2u a1, const v2u (&B)[8]) {
  const f32x4 z = {0.f, 0.f, 0.f, 0.f};
  f32x4 c0 = mfma16(a0, B[0], z), c1 = mfma16(a0, B[1], z), c2 = mfma16(a0, B[2], z), c3 = mfma16(a0, B[3], z);
  c0 = mfma16(a1, B[4], c0); c1 = mfma16(a1, B[5], c1); c2 = mfma16(a1, B[6], c2); c3 = mfma16(a1, B[7], c3);
  return (c0[0] + c1[1]) + (c2[2] + c3[3]);
}
struct Ctx {
  const u16 *vc, *wo, *wg, *wu, *wd; u16* out; unsigned char* ws;
  __amdgpu_buffer_rsrc_t rs; LAS unsigned char* L;
  int lane, w, wv, kg, i16, b, g, c; unsigned E, T, t0, tref, dv;
};

#define LPOST(set) do { asm volatile("" ::: "memory"); *(volatile LAS unsigned*)(L + L_FL + (set) * 16 + w * 4) = T; asm volatile("" ::: "memory"); } while (0)
#define LWAIT4(set) do { for (unsigned spins_ = 0; spins_ < (1u << 24); ++spins_) { const v4u f_ = *(volatile LAS v4u*)(L + L_FL + (set) * 16); \
      if ((f_.x == T) & (f_.y == T) & (f_.z == T) & (f_.w == T)) break; } asm volatile("" ::: "memory"); } while (0)
#define LWAIT1(set, who) do { for (unsigned spins_ = 0; spins_ < (1u << 24); ++spins_) { \
      if (*(volatile LAS unsigned*)(L + L_FL + (set) * 16 + (who) * 4) == T) break; __builtin_amdgcn_s_sleep(1); } asm volatile("" ::: "memory"); } while (0)
#define ONES 0x3f803f80u
#define FL4(set) (*(volatile LAS v4u*)(L + L_FL + (set) * 16))
#define FL4OK(f) (((f).x == T) & ((f).y == T) & ((f).z == T) & ((f).w == T))

__device__ __forceinline__ v4u load_sw(const Ctx& X, int n) {
  const int cb = (X.c & 7) < 7 ? (X.c & 7) : 6;
  if (n < 4) return ldg_nt(X.wo + (size_t)(512 * X.g + 256 * (X.c & 1) + 64 * X.w + 16 * n + (X.lane >> 2)) * DM + 32 * (X.c >> 1) + 8 * (X.lane & 3));
  if (n < 8) return ldg_nt((n >= 6 ? X.wu : X.wg) + (size_t)(128 * (X.c >> 3) + 32 * X.w + 16 * (n & 1) + (X.lane >> 2)) * FF + 224 * X.g + 32 * cb + 8 * (X.lane & 3));
  const int blk = 2 * X.w + (n & 1) < 7 ? 2 * X.w + (n & 1) : 6;
  return ldg_nt(X.wd + (size_t)(224 * X.g + 32 * blk + (X.lane >> 1)) * DM + 16 * X.c + 8 * (X.lane & 1));
}
__device__ __forceinline__ void run_A(Ctx& X, v4u (&Kf)[8], v4u (&Vf)[8], v4u (&Sw)[10]) {
  const int lane = X.lane, w = X.w, kg = X.kg, i16 = X.i16, g = X.g, c = X.c;
  const __amdgpu_buffer_rsrc_t rs = X.rs; LAS unsigned char* L = X.L; LAS unsigned char* stg = L + L_STG + w * STG_W;
  const f32x4 z = {0.f, 0.f, 0.f, 0.f};
  STAMP(12);
  const int u = (lane * 43) >> 7;
  const bool head3 = lane == 3 * u;
  int ooff = head3 ? WS_GRP + g * GRP_SZ + GO_O + w * 12288 + u * 512 + c * 16 : (lane == 1 ? WS_GRP + g * GRP_SZ + GO_ML + w * 512 + c * 16 : OOB);
  const int ostep = (head3 || lane == 1) ? O_L : 0;
  const u16* vb = X.vc + ((size_t)g * SK + (SK - WIN) + 128 * c + 32 * w) * DH;
  {
#pragma unroll
    for (int s = 0; s < 8; ++s) {
      *(LAS v4u*)(stg + (4 * s + kg) * 272 + i16 * 16) = Kf[s];
      SCHED();
      if (s + VPRE < 8) { const int j = s + VPRE; Vf[j] = ldg_nt(vb + (16 * (j >> 2) + 4 * kg + (j & 3)) * DH + 8 * i16); }
      SCHED();
    }
    LGKM0();
#pragma unroll
    for (int s = 0; s < 8; ++s) Kf[s] = *(LAS v4u*)(stg + (16 * (s >> 2) + i16) * 272 + (s & 3) * 64 + kg * 16);
  }
  {
    v4u t[8];
#pragma unroll
    for (int jj = 0; jj < 4; ++jj) {
#pragma unroll
      for (int n = 0; n < 8; ++n) t[n][jj] = PERM(Vf[2 * jj + 1][n >> 1], Vf[2 * jj][n >> 1], (n & 1) ? 0x07060302u : 0x05040100u);
      asm volatile("" : "+v"(t[0][jj]), "+v"(t[7][jj]));
      SCHED();
      Sw[jj] = load_sw(X, jj);
      SCHED();
    }
#pragma unroll
    for (int n = 0; n < 8; ++n) Vf[n] = t[n];
    asm volatile("" :: "v"(Vf[0]), "v"(Vf[1]), "v"(Vf[2]), "v"(Vf[3]), "v"(Vf[4]), "v"(Vf[5]), "v"(Vf[6]), "v"(Vf[7]));
  }
  STAMP(13);
#pragma unroll
  for (int n = 0; n < 4; ++n) { *(LAS v4u*)(L + L_IWO + w * 4096 + n * 1024 + lane * 16) = Sw[n]; SCHED(); Sw[4 + n] = load_sw(X, 4 + n); SCHED(); }
#pragma unroll
  for (int n = 0; n < 4; ++n) {
    *(LAS v4u*)(L + L_IGU + (n >> 1) * 8192 + w * 2048 + (n & 1) * 1024 + lane * 16) = Sw[4 + n]; SCHED();
    if (n & 1) { Sw[8 + (n >> 1)] = load_sw(X, 8 + (n >> 1)); SCHED(); }
  }
  if (DN_LATE == 0) {
#pragma unroll
    for (int n = 0; n < 2; ++n) *(LAS v4u*)(L + L_IDN + (2 * w + n) * 1024 + lane * 16) = Sw[8 + n];
  }
  STAMP(9);
#pragma unroll
  for (int layer = 0; layer < 2; ++layer) {
    BARRIER();
    const unsigned T = __builtin_amdgcn_readfirstlane(*(volatile LAS unsigned*)(L + L_FL));
    {
      v4u qf[4];
#pragma unroll
      for (int s = 0; s < 4; ++s) qf[s] = *(LAS v4u*)(L + L_QB + (i16 >> 2) * QSTR + s * 64 + kg * 16);
      f32x4 sa = z, sb = z;
#pragma unroll
      for (int s = 0; s < 4; ++s) { sa = mfma32(Kf[s], qf[s], sa); sb = mfma32(Kf[4 + s], qf[s], sb); }
      float sv[8];
      if (layer == 0) {
#pragma unroll
        for (int k = 0; k < 8; k += 2) {
          const unsigned p = k < 4 ? pk(sa[k & 3], sa[(k & 3) + 1]) : pk(sb[k & 3], sb[(k & 3) + 1]);
          const unsigned r = pk(bflo(p) * 0.08838834764831845f, bfhi(p) * 0.08838834764831845f);
          sv[k] = bflo(r); sv[k + 1] = bfhi(r);
        }
      } else {
#pragma unroll
        for (int k = 0; k < 4; ++k) { sv[k] = sa[k]; sv[4 + k] = sb[k]; }
      }
      const float CE = layer == 0 ? 1.4426950408889634f : 1.4426950408889634f * 0.08838834764831845f;
      const float m = xrow_max(vmax(vmax(vmax(sv[0], sv[1]), vmax(sv[2], sv[3])), vmax(vmax(sv[4], sv[5]), vmax(sv[6], sv[7])))) * CE;
#pragma unroll
      for (int k = 0; k < 8; ++k) sv[k] = __builtin_amdgcn_exp2f(fmaf(sv[k], CE, -m));
      const v4u pf = {pk(sv[0], sv[1]), pk(sv[2], sv[3]), pk(sv[4], sv[5]), pk(sv[6], sv[7])};
      f32x4 o[8];
#pragma unroll
      for (int n = 0; n < 8; ++n) o[n] = mfma32(pf, Vf[n], z);
      const f32x4 lv = mfma32(pf, v4u{ONES, ONES, ONES, ONES}, z);
      LAS unsigned char* op = L + L_ATT + w * 2048 + kg * 512 + i16 * 32;
      *(LAS f32x4*)op = f32x4{o[0][0], o[1][1], o[2][2], o[3][3]};
      *(LAS f32x4*)(op + 16) = f32x4{o[4][0], o[5][1], o[6][2], o[7][3]};
      if (i16 == 4 * kg) *(LAS f32x2*)(L + L_ML + kg * 32 + w * 8) = f32x2{m, lv[0]};
    }
    LPOST(1 + layer);
    const unsigned tm = rtc();
    {
      f32x4 ml0, ml1; f32x2 ov[4];
      for (unsigned sp = 0; sp < (1u << 24); ++sp) {
        const v4u f = FL4(1 + layer);
        ml0 = *(volatile LAS f32x4*)(L + L_ML + w * 32); ml1 = *(volatile LAS f32x4*)(L + L_ML + w * 32 + 16);
#pragma unroll
        for (int k = 0; k < 4; ++k) ov[k] = *(volatile LAS f32x2*)(L + L_ATT + k * 2048 + w * 512 + lane * 8);
        if (FL4OK(f)) break;
      }
      const float M = vmax(vmax(ml0[0], ml0[2]), vmax(ml1[0], ml1[2]));
      const float e0 = __builtin_amdgcn_exp2f(ml0[0] - M), e1 = __builtin_amdgcn_exp2f(ml0[2] - M), e2 = __builtin_amdgcn_exp2f(ml1[0] - M), e3 = __builtin_amdgcn_exp2f(ml1[2] - M);
      const f32x2 a = (ov[0] * e0 + ov[1] * e1) + (ov[2] * e2 + ov[3] * e3);
      const float ls = fmaf(e0, ml0[1], e1 * ml0[3]) + fmaf(e2, ml1[1], e3 * ml1[3]);
      unsigned dv = pk(a[0], a[1]);
      unsigned d1 = dppu<0x130>(dv);
      unsigned d2 = dppu<0x130>(d1);
      dv = head3 ? dv : __float_as_uint(M); d1 = head3 ? d1 : __float_as_uint(ls); d2 = head3 ? d2 : tm;
      st_sc1(rs, ooff, v4u{dv, d1, d2, T});
      ooff += ostep; asm volatile("" : "+v"(ooff));
    }
    if (layer == 0) { STAMP(3);
      if (DN_LATE) {
#pragma unroll
        for (int n = 0; n < 2; ++n) *(LAS v4u*)(L + L_IDN + (2 * w + n) * 1024 + lane * 16) = Sw[8 + n];
      }
      LPOST(15);
    } else STAMP(15);
  }
}

#define RETRY(pi, reload) if (LIKELY(__all(ok)) || spins > SPIN_CAP) break; asm volatile("" ::: "memory"); DELAY(8); PASS(pi); if (!ok) { reload; }
#define SLIP(i, lat) do { const int late_ = (int)(rtc() - (X.tref + (unsigned)DLV(i) + (unsigned)(lat))); if (late_ > 0) X.tref += (unsigned)(late_ + DLV(15)); } while (0)
#define RESYNC(tmax, i, nom) do { const int late_ = (int)((tmax) - (X.tref + (unsigned)DLV(i) + (unsigned)(nom))); if (late_ > 0) X.tref += (unsigned)late_; } while (0)
#define QUAD_TMAX(tv) ({ int dt_ = (int)((tv) - X.tref); dt_ = max(dt_, (int)dppu<XOR1>((unsigned)dt_)); dt_ = max(dt_, (int)dppu<XOR2>((unsigned)dt_)); X.tref + (unsigned)__builtin_amdgcn_readfirstlane(dt_); })
#define LN_STATS(ds) float s1 = __uint_as_float((ds).x), s2 = __uint_as_float((ds).y); \
      s1 += dpp<XOR1>(s1); s2 += dpp<XOR1>(s2); s1 += dpp<XOR2>(s1); s2 += dpp<XOR2>(s2); \
      const float mean = s1 * (1.f / DM), rstd = __builtin_amdgcn_rsqf(fmaxf(fmaf(-mean, mean, s2 * (1.f / DM)), 0.f) + 1e-5f)

template <int layer>
__device__ __forceinline__ void run_B(Ctx& X, const v2u (&Bq)[8], v4u (&Aw)[12], v2u (&Bg)[8], v4u (&Bd)[7], float& xr, f32x4& csq, float (&csg)[2]) {
  const int lane = X.lane, w = X.w, kg = X.kg, i16 = X.i16, g = X.g, c = X.c, kq = c >> 3;
  const unsigned T = X.T; const __amdgpu_buffer_rsrc_t rs = X.rs; LAS unsigned char* L = X.L;
  LAS unsigned char* red = L + L_RED + layer * RED_SZ;
  const f32x4 z = {0.f, 0.f, 0.f, 0.f};
  const int grp = WS_GRP + g * GRP_SZ;
  v2u fy0; v4u c0, c1; unsigned tq;
  const int qun = kg * 8 + (lane & 7), qidx = ((lane >> 3) & 1) * 2048 + (32 * w + qun) * 16;
  const int xun = 8 * w + (lane & 7);
  {
    int qoff = (layer == 0 ? WS_Q0 + g * 8192 : grp + GO_Q1) + qidx, soff = grp + GO_ST2 + (lane & 3) * 16;
    PIN(qoff); PIN(soff);
    if (layer == 0) WAIT_AT(X.t0, 11); else WAIT_AT(X.tref, 4);
    v4u ds = v4u{0u, 0u, 0u, T}, r0, r1;
#define QLOADS() do { if (layer == 1) ds = ld_sc1(rs, soff); r0 = ld_sc1(rs, qoff); r1 = ld_sc1(rs, qoff + 4096); } while (0)
    QLOADS();
    SCHED();
    for (unsigned spins = 0;; ++spins) {
      LN_STATS(ds);
      const v4u d0 = unF(r0, T), d1 = unF(r1, T);
      float qv[4];
#pragma unroll
      for (int k = 0; k < 4; ++k) {
        float t = __uint_as_float(d0[k]) + __uint_as_float(d1[k]); t += dpp<ROR8>(t);
        qv[k] = layer == 0 ? t : rstd * fmaf(-mean, csq[k], t);
      }
      *(LAS v2u*)(L + L_QB + w * QSTR + qun * 8) = v2u{pk(qv[0], qv[1]), pk(qv[2], qv[3])};
      const bool ok = (((or4(d0) | or4(d1)) & 0xffu) == 0u) & (ds.w == T);
      RETRY(layer ? 7 : 0, QLOADS());
    }
    tq = ds.z;
    STAMP(2 + 12 * layer);
  }
  BARRIER();
  if (layer == 1) { RESYNC(QUAD_TMAX(tq), 3, NOM_Q); SLIP(4, 72); }
  if (layer == 0) {
    tq = rtc();
    if (w == TW && lane == 0) { st2_sc1(rs, WS_FO + (g * 32 + c) * 128, v2u{T, tq}); st_sc1(rs, WS_FO + (g * 32 + c) * 128 + 64, v4u{T, X.t0, tq, X.dv}); }
    fy0 = ld2_sc1(rs, w == TW ? WS_FO + (g * 32 + (lane & 31)) * 128 : OOB);
    c0 = ld_sc1(rs, WS_Q0 + 65536 + g * 8192 + qidx); c1 = ld_sc1(rs, WS_Q0 + 65536 + g * 8192 + qidx + 4096);
    SCHED();
  }
#define WO_PREP() do { const int hh = w >> 1; \
_Pragma("unroll") \
    for (int t = 0; t < 6; ++t) { \
      const int un = 4 * t + kg, r0 = 6 * un + (i16 >> 2); \
      const int ra = r0 > 127 ? 127 : r0, rb = r0 + 4 > 127 ? 127 : r0 + 4; \
_Pragma("unroll") \
      for (int ct = 0; ct < 2; ++ct) { \
        const v2u lo = trread(L + L_IWO + (hh * 128 + ra) * 64 + ct * 32 + (i16 & 3) * 8), hi = trread(L + L_IWO + (hh * 128 + rb) * 64 + ct * 32 + (i16 & 3) * 8); \
        Aw[t * 2 + ct] = v4u{(t == 5 && un > 21) ? 0u : lo.x, (t == 5 && un > 20) ? 0u : lo.y, (t == 5 && un > 20) ? 0u : hi.x, 0u}; \
      } \
    } \
  } while (0)
  if (layer == 0) {
    WO_PREP();
#pragma unroll
    for (int u = 0; u < 8; ++u) Bg[u] = trread(L + L_IGU + (w >> 1) * 8192 + (64 * (u >> 2) + 16 * kg + 4 * (u & 3) + (i16 >> 2)) * 64 + (w & 1) * 32 + (i16 & 3) * 8);
    const v2u one = {ONES, ONES};
    const float cs = gemv128(one, one, Bg);
    const float p1 = dpp<0x55>(cs), p2 = dpp<0xAA>(cs), p3 = dpp<0xFF>(cs);
    st_sc1(rs, ((lane & 0x33) == 0 && (c & 7) < 7) ? WS_GC + g * 8192 + kq * 2048 + (w >> 1) * 1024 + (8 * (c & 7) + 4 * (w & 1) + (i16 >> 2)) * 16 : OOB, mkF(cs, p1, p2, p3, T));

  }
  {
    const int hh = w >> 1, st = w & 1, h = 2 * (c & 1) + hh;
    int obase = grp + GO_O + layer * O_L + h * 12288 + (16 * st + i16) * 16 + kg * 512;
    int o5 = obase + 5 * 2048 - (kg & 2) * 512;
    const int rot = (4 * kg + i16) & 15;
    int mlA = grp + GO_ML + layer * O_L + h * 512 + (16 * st + rot) * 16, mlB = mlA ^ 256;
    const int slot = ((lane >> 4) & 1) * 4 + (lane & 3), rep = ((lane >> 2) & 3) + 4 * (lane >> 5);
    int yst = WS_Y + layer * 262144 + rep * 32768 + (2 * g + (c & 1)) * 2048 + (c >> 1) * 128 + slot * 16;
    PIN(obase); PIN(o5); PIN(mlA); PIN(mlB); PIN(yst);
    if (layer == 0) {
      unsigned tg;
      if (w == TW) {
        for (unsigned spins = 0;; ++spins) {
          if ((lane & 31) == c) fy0 = v2u{T, tq};
          if (__all(fy0.x == T) || spins > SPIN_CAP) break;
          asm volatile("" ::: "memory"); DELAY(10); PASS(1);
          fy0 = ld2_sc1(rs, WS_FO + (g * 32 + (lane & 31)) * 128);
        }
        int dt = (int)(fy0.y - X.t0); dt = max(dt, (int)dppu<XOR1>((unsigned)dt)); dt = max(dt, (int)dppu<XOR2>((unsigned)dt)); dt = max(dt, (int)dppu<HMIR>((unsigned)dt));
        dt = max(dt, (int)dppu<MIR>((unsigned)dt)); dt = max(__builtin_amdgcn_readlane(dt, 0), __builtin_amdgcn_readlane(dt, 16)); tg = X.t0 + (unsigned)dt;
        *(volatile LAS unsigned*)(L + L_FL + 3 * 16) = tg; LPOST(3);
      } else { LWAIT1(3, TW); tg = __builtin_amdgcn_readfirstlane(*(volatile LAS unsigned*)(L + L_FL + 3 * 16)); }
      if (c == 0 && w == TW && lane < 8) st2_sc1(rs, WS_FY + (lane * 8 + g) * 128, v2u{T, tg});
      for (unsigned spins = 0;; ++spins) {
        c0 = unF(c0, T); c1 = unF(c1, T);
        if (__all(((or4(c0) | or4(c1)) & 0xffu) == 0u) || spins > SPIN_CAP) break;
        asm volatile("" ::: "memory"); DELAY(8); c0 = ld_sc1(rs, WS_Q0 + 65536 + g * 8192 + qidx); c1 = ld_sc1(rs, WS_Q0 + 65536 + g * 8192 + qidx + 4096);
      }
#pragma unroll
      for (int k = 0; k < 4; ++k) { float t = __uint_as_float(c0[k]) + __uint_as_float(c1[k]); t += dpp<ROR8>(t); csq[k] = t; }
      { unsigned t_ = tg + (unsigned)DLV(0); for (int k_ = 0; k_ < 4096 && (int)(rtc() - t_) < 0; ++k_) {} }
      X.tref = tg;
    } else WAIT_AT(X.tref, 5);
    if (layer == 0) fy0 = ld2_sc1(rs, WS_FY + (g * 8 + (lane & 7)) * 128);
    v4u d[8];
#define OLOADS() do { d[6] = ld_sc1(rs, mlA); d[7] = ld_sc1(rs, mlB); _Pragma("unroll") for (int t = 0; t < 5; ++t) d[t] = ld_sc1(rs, obase + t * 2048); d[5] = ld_sc1(rs, o5); } while (0)
    OLOADS();
    SCHED();
    for (unsigned spins = 0;; ++spins) {
      const float mA = __uint_as_float(d[6].x), lA = __uint_as_float(d[6].y), mB = __uint_as_float(d[7].x), lB = __uint_as_float(d[7].y);
      const float M = row_max(vmax(mA, mB));
      const float eA = __builtin_amdgcn_exp2f(mA - M), eB = __builtin_amdgcn_exp2f(mB - M);
      const float Ls = row_sum(fmaf(eA, lA, eB * lB));
      const float wgt = eA * __builtin_amdgcn_rcpf(Ls);
      const float w0 = dpp<0x150>(wgt), w1 = dpp<0x151>(wgt), w2 = dpp<0x152>(wgt), w3 = dpp<0x153>(wgt);
      f32x4 a0 = z, a1 = z;
#pragma unroll
      for (int t = 0; t < 6; ++t) { a0 = mfma32(d[t], Aw[2 * t], a0); a1 = mfma32(d[t], Aw[2 * t + 1], a1); }
      const float y0 = fmaf(a0[0], w0, a0[1] * w1) + fmaf(a0[2], w2, a0[3] * w3), y1 = fmaf(a1[0], w0, a1[1] * w1) + fmaf(a1[2], w2, a1[3] * w3);
      auto sw = __builtin_amdgcn_permlane16_swap(__float_as_uint(y0), __float_as_uint(y1), false, false);
      const float yy = __uint_as_float(sw[0]) + __uint_as_float(sw[1]);
      auto sx = __builtin_amdgcn_permlane32_swap(__float_as_uint(yy), __float_as_uint(yy), false, false);
      *(LAS float*)(red + w * RED_ROW + (lane & 31) * 4) = __uint_as_float(sx[0]) + __uint_as_float(sx[1]);
      const bool ok = (d[0].w == T) & (d[1].w == T) & (d[2].w == T) & (d[3].w == T) & (d[4].w == T) & (d[5].w == T) & (d[6].w == T) & (d[7].w == T);
      RETRY(layer ? 8 : 1, OLOADS());
    }
    if (layer == 0) asm volatile("s_waitcnt vmcnt(0)" ::: "memory");
    LPOST(4 + 5 * layer);
    STAMP(4 + 12 * layer);
    if (w == TW) {
      f32x4 v;
      for (unsigned sp = 0; sp < (1u << 24); ++sp) {
        const v4u f = FL4(4 + 5 * layer);
        v = *(volatile LAS f32x4*)(red + ((lane >> 2) & 3) * RED_ROW + slot * 16);
        if (FL4OK(f)) break;
      }
#pragma unroll
      for (int k = 0; k < 4; ++k) { float t = v[k]; t += dpp<ROR8>(t); t += dpp<ROR4>(t); v[k] = t; }
      st_sc1(rs, yst, mkF(v[0], v[1], v[2], v[3], T));
    }
    if (layer == 1) {
      int dt = max((int)(d[6].z - X.tref), (int)(d[7].z - X.tref));
      dt = max(dt, (int)dppu<XOR1>((unsigned)dt)); dt = max(dt, (int)dppu<XOR2>((unsigned)dt)); dt = max(dt, (int)dppu<HMIR>((unsigned)dt)); dt = max(dt, (int)dppu<MIR>((unsigned)dt));
      RESYNC(X.tref + (unsigned)__builtin_amdgcn_readfirstlane(dt), 4, NOM_O);
      SLIP(5, 125);
    }
  }
  STAMP(5 + 12 * layer);
#define X_SHARE(xb, setx, sets) do { LPOST(setx); \
    float s1 = xr, s2 = xr * xr; \
    s1 += dpp<XOR1>(s1); s2 += dpp<XOR1>(s2); s1 += dpp<XOR2>(s1); s2 += dpp<XOR2>(s2); s1 += dpp<HMIR>(s1); s2 += dpp<HMIR>(s2); \
    *(LAS f32x2*)((xb) + 256 + (4 * w + kg) * 8) = f32x2{s1, s2}; LPOST(sets); } while (0)
#define X_FETCH(xb, setx) v2u a0, a1; for (unsigned sp = 0; sp < (1u << 24); ++sp) { const v4u f = FL4(setx); \
      a0 = *(volatile LAS v2u*)((xb) + (4 * kg + (i16 & 3)) * 8); a1 = *(volatile LAS v2u*)((xb) + 128 + (4 * kg + (i16 & 3)) * 8); if (FL4OK(f)) break; }
#define STATS_PUBLISH(xb, sets, idx) do { f32x2 sp2; for (unsigned sp = 0; sp < (1u << 24); ++sp) { const v4u f = FL4(sets); sp2 = *(volatile LAS f32x2*)((xb) + 256 + i16 * 8); if (FL4OK(f)) break; } \
    const float S1 = row_sum(sp2[0]), S2 = row_sum(sp2[1]); \
    st_sc1(rs, lane == 0 ? grp + (idx) + kq * 16 : OOB, v4u{__float_as_uint(S1), __float_as_uint(S2), rtc(), T}); } while (0)
  {
    const int go = kq * 2048 + (w >> 1) * 1024 + (8 * (c & 7) + 4 * (w & 1) + (i16 >> 2)) * 16;
    const bool gst = (lane & 0x33) == 0 && (c & 7) < 7;
    int goff = gst ? grp + GO_G + layer * O_L + go : OOB;
    int yoff = WS_Y + layer * 262144 + g * 32768 + (lane >> 3) * 2048 + (32 * kq + xun) * 16;
    PIN(goff); PIN(yoff);
    if (layer == 0) {
      {
        for (unsigned spins = 0;; ++spins) {
          if ((lane & 7) == g) fy0 = v2u{T, X.tref};
          if (__all(fy0.x == T) || spins > SPIN_CAP) break;
          asm volatile("" ::: "memory"); DELAY(14); PASS(3);
          fy0 = ld2_sc1(rs, WS_FY + (g * 8 + (lane & 7)) * 128);
        }
        int dt = (int)(fy0.y - X.t0); dt = max(dt, (int)dppu<XOR1>((unsigned)dt)); dt = max(dt, (int)dppu<XOR2>((unsigned)dt)); dt = max(dt, (int)dppu<HMIR>((unsigned)dt));
        X.tref = X.t0 + (unsigned)__builtin_amdgcn_readfirstlane(dt);
      }
      { unsigned t_ = X.tref + (unsigned)DLV(1); for (int k_ = 0; k_ < 4096 && (int)(rtc() - t_) < 0; ++k_) {} }
    } else WAIT_AT(X.tref, 6);
    LAS unsigned char* xb = L + L_XB + 2 * layer * XB_SZ;
    float xn;
    v4u r0 = ld_sc1(rs, yoff), r1 = ld_sc1(rs, yoff + 16384);
    SCHED();
    for (unsigned spins = 0;; ++spins) {
      const v4u d0 = unF(r0, T), d1 = unF(r1, T);
      float t = rows_reduce4(__builtin_bit_cast(f32x4, d0) + __builtin_bit_cast(f32x4, d1));
      t += dpp<ROR8>(t);
      const unsigned p = pk(xr + t, 0.f);
      *(LAS u16*)(xb + (4 * xun + kg) * 2) = (u16)p;
      xn = bflo(p);
      const bool ok = ((or4(d0) | or4(d1)) & 0xffu) == 0u;
      RETRY(layer ? 9 : 4, r0 = ld_sc1(rs, yoff); r1 = ld_sc1(rs, yoff + 16384));
    }
    xr = xn;
    STAMP(6 + 12 * layer);
    X_SHARE(xb, 6 + 4 * layer, 12 + layer);
    if ((c & 7) < 7) {
      X_FETCH(xb, 6 + 4 * layer);
      const float part = gemv128(a0, a1, Bg);
      const float p1 = dpp<0x55>(part), p2 = dpp<0xAA>(part), p3 = dpp<0xFF>(part);
      st_sc1(rs, goff, mkF(part, p1, p2, p3, T));
    } else if (w == 0) STATS_PUBLISH(xb, 12 + layer, GO_ST + layer * O_L);
  }
  if (layer == 1) STAMP(19);
  SLIP(layer == 0 ? 1 : 6, 98);
  if (layer == 0) {
    const v4u zz = {0u, 0u, 0u, 0u};
    const int slot = ((lane >> 4) & 1) * 4 + (lane & 3), rep = ((lane >> 2) & 3) + 4 * (lane >> 5);
    if (w == TW) {
      st_sc1(rs, WS_Y + 262144 + rep * 32768 + (2 * g + (c & 1)) * 2048 + (c >> 1) * 128 + slot * 16, zz);
      st_sc1(rs, i16 < 8 ? WS_X + 131072 + i16 * 16384 + g * 2048 + c * 64 + kg * 16 : OOB, zz);
    }
    st_sc1(rs, lane < 30 ? grp + O_L + (c * 4 + w) * 480 + lane * 16
                         : ((lane & 0x33) == 0x20 ? grp + GO_Q1 + kq * 2048 + ((c & 7) * 64 + w * 16 + i16) * 4 : ((lane == 63 && (c & 7) == 0 && w == 0) ? grp + GO_ST2 + kq * 16 : OOB)), zz);
  }
  {
    const int hu = 14 * w + (i16 < 14 ? i16 : 13);
    const int gi = kg * 2048 + hu * 16;
    int gld = grp + GO_G + layer * O_L + gi, soff = grp + GO_ST + layer * O_L + (lane & 3) * 16;
    int xst = i16 < 8 ? WS_X + layer * 131072 + i16 * 16384 + g * 2048 + c * 64 + kg * 16 : OOB;
    PIN(gld); PIN(soff); PIN(xst);
    if (layer == 0 && w == TW) {
      LWAIT4(15);
#pragma unroll
      for (int t = 0; t < 7; ++t) {
        const int r0 = 32 * t + 8 * kg + (i16 >> 2);
        const v2u lo = trread(L + L_IDN + r0 * 32 + (i16 & 3) * 8), hi = trread(L + L_IDN + (r0 + 4) * 32 + (i16 & 3) * 8);
        Bd[t] = v4u{lo.x, lo.y, hi.x, hi.y};
      }
    }
    WAIT_AT(X.tref, layer == 0 ? 2 : 7);
    LAS unsigned char* hb = L + L_HB + layer * 512;
    v4u ds, r0, r1;
#define GLOADS() do { ds = ld_sc1(rs, soff); if (layer == 0) { c0 = ld_sc1(rs, WS_GC + g * 8192 + gi); c1 = ld_sc1(rs, WS_GC + g * 8192 + gi + 1024); } \
      r0 = ld_sc1(rs, gld); r1 = ld_sc1(rs, gld + 1024); } while (0)
    GLOADS();
    SCHED();
    for (unsigned spins = 0;; ++spins) {
      LN_STATS(ds);
      unsigned cbad = 0u;
      if (layer == 0) {
        const v4u e0 = unF(c0, T), e1 = unF(c1, T); cbad = or4(e0) | or4(e1);
        csg[0] = rows_reduce4(__builtin_bit_cast(f32x4, e0)); csg[1] = rows_reduce4(__builtin_bit_cast(f32x4, e1));
      }
      const v4u d0 = unF(r0, T), d1 = unF(r1, T);
      const float gv = rstd * fmaf(-mean, csg[0], rows_reduce4(__builtin_bit_cast(f32x4, d0)));
      const float uv = rstd * fmaf(-mean, csg[1], rows_reduce4(__builtin_bit_cast(f32x4, d1)));
      const float hv = uv * __builtin_amdgcn_rcpf(1.f + __builtin_amdgcn_exp2f(-1.4426950408889634f * gv));
      *(LAS u16*)(hb + (4 * hu + kg) * 2) = (u16)pk(hv, 0.f);
      const bool ok = (((or4(d0) | or4(d1) | cbad) & 0xffu) == 0u) & (ds.w == T);
      RETRY(5, GLOADS());
    }
    if (layer == 0) asm volatile("s_waitcnt vmcnt(0)" ::: "memory");
    LPOST(7 + 4 * layer);
    STAMP(8 + 12 * layer);
    if (w == TW) {
      v4u hf[7];
      for (unsigned sp = 0; sp < (1u << 24); ++sp) {
        const v4u f = FL4(7 + 4 * layer);
#pragma unroll
        for (int t = 0; t < 7; ++t) hf[t] = *(volatile LAS v4u*)(hb + t * 64 + kg * 16);
        if (FL4OK(f)) break;
      }
      f32x4 a0 = z, a1 = z;
#pragma unroll
      for (int t = 0; t < 7; ++t) { if (t & 1) a1 = mfma32(Bd[t], hf[t], a1); else a0 = mfma32(Bd[t], hf[t], a0); }
      a0 += a1;
      st_sc1(rs, xst, mkF(a0[0], a0[1], a0[2], a0[3], T));
    }
    if (layer == 0) STAMP(10);
    RESYNC(QUAD_TMAX(ds.z), layer == 0 ? 1 : 6, NOM_G);
    SLIP(layer == 0 ? 2 : 7, layer == 0 ? 125 : 105);
  }
  if (layer == 0 || (g == 0 && (c & 7) == 0)) {
    int xoff = WS_X + layer * 131072 + g * 16384 + (lane >> 3) * 2048 + (32 * kq + xun) * 16;
    int qst = (lane & 0x33) == 0 ? grp + GO_Q1 + kq * 2048 + ((c & 7) * 64 + w * 16 + i16) * 4 : OOB;
    PIN(xoff); PIN(qst);
    WAIT_AT(X.tref, layer == 0 ? 3 : 9);
    LAS unsigned char* xb = L + L_XB + XB_SZ;
    unsigned p;
    v4u r0 = ld_sc1(rs, xoff);
    for (unsigned spins = 0;; ++spins) {
      const v4u d0 = unF(r0, T);
      float t = rows_reduce4(__builtin_bit_cast(f32x4, d0));
      t += dpp<ROR8>(t);
      p = pk(xr + t, 0.f);
      if (layer == 0) *(LAS u16*)(xb + (4 * xun + kg) * 2) = (u16)p;
      const bool ok = (or4(d0) & 0xffu) == 0u;
      RETRY(6, r0 = ld_sc1(rs, xoff));
    }
    if (layer == 0) {
      xr = bflo(p);
      X_SHARE(xb, 8, 14);
      X_FETCH(xb, 8);
      const float part = gemv128(a0, a1, Bq);
      const float p1 = dpp<0x55>(part), p2 = dpp<0xAA>(part), p3 = dpp<0xFF>(part);
      st_sc1(rs, qst, mkF(part, p1, p2, p3, T));
      if ((c & 7) == 0 && w == 0) STATS_PUBLISH(xb, 14, GO_ST2);
      STAMP(11);
      SLIP(3, 100);
    } else {
      if ((lane & 8) == 0) *(GAS u16*)(X.out + 128 * kq + 4 * xun + kg) = (u16)p;
      if (c == 0 && w == 0 && lane == 0) st1_sc1(rs, 0, X.E + 1u);
      if (c == 0 && w == 0) {
        int mn = 1 << 30, mx = -(1 << 30), mdur = 0, mq = 0, mindur = 1 << 30;
        for (int k = 0; k < 4; ++k) { const v4u f = ld_sc1(rs, WS_FO + (k * 64 + lane) * 128 + 64); const int a = (int)(f.y - X.t0); mn = min(mn, a); mx = max(mx, a); mdur = max(mdur, (int)(f.z - f.y)); mindur = min(mindur, (int)(f.z - f.y)); mq = max(mq, (int)(f.w - f.y)); }
        for (int o = 32; o >= 1; o >>= 1) { mn = min(mn, __shfl_xor(mn, o)); mx = max(mx, __shfl_xor(mx, o)); mdur = max(mdur, __shfl_xor(mdur, o)); mindur = min(mindur, __shfl_xor(mindur, o)); mq = max(mq, __shfl_xor(mq, o)); }
        const int dv = __builtin_amdgcn_readfirstlane(DIAG == 1 ? mx - mn : DIAG == 2 ? -mn : DIAG == 3 ? mdur : DIAG == 4 ? mq : DIAG == 5 ? mindur : 0);
        const unsigned e_ = rtc() + (unsigned)dv; for (int k_ = 0; k_ < 100000 && (int)(rtc() - e_) < 0; ++k_) {}
      }
      STAMP(21);
    }
  }
}

__global__ void __launch_bounds__(512, 2)
fused_decode(const u16* x, const u16* kc, const u16* vc, const u16* wq, const u16* wo, const u16* wg, const u16* wu, const u16* wd, u16* out, unsigned char* ws) {
  extern __shared__ __attribute__((aligned(16))) unsigned char smem[];
  Ctx X;
  const int tid = threadIdx.x;
  X.lane = tid & 63; X.wv = __builtin_amdgcn_readfirstlane(tid >> 6); X.w = X.wv & 3; X.kg = X.lane >> 4; X.i16 = X.lane & 15;
  X.b = blockIdx.x; X.g = X.b & 7; X.c = X.b >> 3;
  X.t0 = rtc();
  unsigned pfv;
  {
    unsigned long long pc; unsigned len;
    asm volatile("s_getpc_b64 %0\n\tLpf_here:\n\ts_mov_b32 %1, Lpf_end - Lpf_here" : "=s"(pc), "=s"(len));
    unsigned off = (unsigned)(X.c * 8 + X.wv) * 128u; off = off < len ? off : 0u;
    pfv = *(const GAS unsigned*)(pc + off);
  }
  SCHED();
  asm volatile("" : "+s"(x), "+s"(kc), "+s"(vc), "+s"(wq), "+s"(wo), "+s"(wg), "+s"(wu), "+s"(wd), "+s"(out), "+s"(ws));
  SCHED();
  X.vc = vc; X.wo = wo; X.wg = wg; X.wu = wu; X.wd = wd; X.out = out; X.ws = ws;
  X.L = (LAS unsigned char*)smem;
  X.rs = __builtin_amdgcn_make_buffer_rsrc((void*)ws, 0, WS_BYTES, 0x00020000);
  const int lane = X.lane, w = X.w, kg = X.kg, i16 = X.i16, g = X.g, c = X.c, kq = c >> 3;
  const __amdgpu_buffer_rsrc_t rs = X.rs; LAS unsigned char* L = X.L;
  v4u Kf[8], Vf[8], Sw[10], wr[4], xf; v2u xa, xb; unsigned xu, E;
  if (X.wv >= 4) {
    DELAY(12);
    const size_t kvbase = ((size_t)g * SK + (SK - WIN) + 128 * c + 32 * w) * DH;
#pragma unroll
    for (int s = 0; s < 8; ++s) Kf[s] = ldg_nt(kc + kvbase + s * 512 + lane * 8);
#pragma unroll
    for (int j = 0; j < VPRE; ++j) Vf[j] = ldg_nt(vc + kvbase + (16 * (j >> 2) + 4 * kg + (j & 3)) * DH + 8 * i16);
  } else {
#pragma unroll
    for (int n = 0; n < 4; ++n) wr[n] = ldg_nt(wq + (size_t)(128 * kq + 32 * w + 8 * n + (lane >> 3)) * 4096 + 512 * g + 64 * (c & 7) + 8 * (lane & 7));
    xf = *(const GAS v4u*)(x + 8 * lane);
    xa = *(const GAS v2u*)(x + 4 * (32 * kq + 4 * kg + (i16 & 3))); xb = *(const GAS v2u*)(x + 4 * (32 * kq + 16 + 4 * kg + (i16 & 3)));
    xu = *(const GAS u16*)(x + 128 * kq + 4 * (8 * w + (lane & 7)) + kg);
    E = ld1_sc1(rs, 0);
    SCHED();
    {
      const v4u zz = {0u, 0u, 0u, 0u};
      const int slot = ((lane >> 4) & 1) * 4 + (lane & 3), rep = ((lane >> 2) & 3) + 4 * (lane >> 5), grp = WS_GRP + g * GRP_SZ;
      if (w == TW) {
        st_sc1(rs, WS_Y + rep * 32768 + (2 * g + (c & 1)) * 2048 + (c >> 1) * 128 + slot * 16, zz);
        st_sc1(rs, i16 < 8 ? WS_X + i16 * 16384 + g * 2048 + c * 64 + kg * 16 : OOB, zz);
      }
      st_sc1(rs, (c & 7) < 7 ? ((lane & 0x33) == 0 ? grp + GO_G + kq * 2048 + (w >> 1) * 1024 + (8 * (c & 7) + 4 * (w & 1) + (i16 >> 2)) * 16 : OOB)
                             : ((w == 0 && lane == 0) ? grp + GO_ST + kq * 16 : OOB), zz);
    }
  }
  SCHED();
  int wv2 = X.wv; asm volatile("" : "+s"(wv2));
  if (wv2 >= 4) run_A(X, Kf, Vf, Sw);
  int wv3 = X.wv; asm volatile("" : "+s"(wv3));
  if (wv3 < 4) {
#pragma unroll
    for (int n = 0; n < 4; ++n) *(LAS v4u*)(L + L_IMG + w * 4096 + n * 1024 + lane * 16) = wr[n];
    const unsigned hsh = (E + 1u) * 0x9E3779B1u;
    const unsigned T = __builtin_amdgcn_readfirstlane(((hsh & 0xbfffbf00u) | (((hsh >> 8) & 0xbfu) ^ 0xa5u)));
    X.E = E; X.T = T;
    LPOST(0);
    float xr = bflo(xu);
    v2u Bq[8], Bg[8]; v4u Aw[12], Bd[7];
    {
      float xv[8] = {bflo(xf.x), bfhi(xf.x), bflo(xf.y), bfhi(xf.y), bflo(xf.z), bfhi(xf.z), bflo(xf.w), bfhi(xf.w)};
      float s = 0.f;
#pragma unroll
      for (int k = 0; k < 8; ++k) s += xv[k];
      const float mean = xrow_sum(row_sum(s)) * (1.f / DM);
      float v = 0.f;
#pragma unroll
      for (int k = 0; k < 8; ++k) { const float d = xv[k] - mean; v = fmaf(d, d, v); }
      const float var = xrow_sum(row_sum(v)) * (1.f / DM);
      const float mb = rbf(mean), isd = 1.0f / rbf(sqrtf(rbf(rbf(var) + 1e-5f)));
      const float xi[8] = {bflo(xa.x), bfhi(xa.x), bflo(xa.y), bfhi(xa.y), bflo(xb.x), bfhi(xb.x), bflo(xb.y), bfhi(xb.y)};
      float xn[8];
#pragma unroll
      for (int k = 0; k < 8; ++k) xn[k] = rbf(xi[k] - mb) * isd;
      const v2u a0 = {pk(xn[0], xn[1]), pk(xn[2], xn[3])}, a1 = {pk(xn[4], xn[5]), pk(xn[6], xn[7])};
      LWAIT4(0);
#pragma unroll
      for (int u = 0; u < 8; ++u) Bq[u] = trread(L + L_IMG + (64 * (u >> 2) + 16 * kg + 4 * (u & 3) + (i16 >> 2)) * 128 + w * 32 + (i16 & 3) * 8);
      const int qo = (lane & 0x33) == 0 ? WS_Q0 + g * 8192 + kq * 2048 + ((c & 7) * 64 + w * 16 + i16) * 4 : OOB - 65536;
      {
        const float part = gemv128(a0, a1, Bq);
        const float p1 = dpp<0x55>(part), p2 = dpp<0xAA>(part), p3 = dpp<0xFF>(part);
        st_sc1(rs, qo, mkF(part, p1, p2, p3, T));
      }
      STAMP(1);
      X.dv = rtc();
      {
        const v2u one = {ONES, ONES};
        const float part = gemv128(one, one, Bq);
        const float p1 = dpp<0x55>(part), p2 = dpp<0xAA>(part), p3 = dpp<0xFF>(part);
        st_sc1(rs, qo + 65536, mkF(part, p1, p2, p3, T));
      }
    }
    f32x4 csq = {0.f, 0.f, 0.f, 0.f}; float csg[2] = {0.f, 0.f};
    run_B<0>(X, Bq, Aw, Bg, Bd, xr, csq, csg);
    run_B<1>(X, Bq, Aw, Bg, Bd, xr, csq, csg);
  }
  asm volatile("" :: "v"(pfv));
  STAMP_FLUSH();
  asm volatile("Lpf_end:");
}

extern "C" void kernel_launch(void* const* d_in, const int* in_sizes, int n_in, void* d_out, int out_size, void* d_ws, size_t ws_size,
                              hipStream_t stream) {
  (void)in_sizes; (void)n_in; (void)out_size; (void)ws_size;
  (void)hipFuncSetAttribute((const void*)fused_decode, hipFuncAttributeMaxDynamicSharedMemorySize, LDS_BYTES);
  hipLaunchKernelGGL(fused_decode, dim3(256), dim3(512), LDS_BYTES, stream, (const u16*)d_in[0], (const u16*)d_in[1], (const u16*)d_in[2],
                     (const u16*)d_in[3], (const u16*)d_in[4], (const u16*)d_in[6], (const u16*)d_in[7], (const u16*)d_in[8], (u16*)d_out,
                     (unsigned char*)d_ws);
}
```

```cpp
#define DIAG 0
#include <hip/hip_runtime.h>
#include <stdint.h>

typedef unsigned short u16;
typedef unsigned v4u __attribute__((ext_vector_type(4)));
typedef unsigned v2u __attribute__((ext_vector_type(2)));
typedef float f32x4 __attribute__((ext_vector_type(4)));
typedef float f32x2 __attribute__((ext_vector_type(2)));
typedef short s16x4 __attribute__((ext_vector_type(4)));
typedef __bf16 bf16x8 __attribute__((ext_vector_type(8)));
typedef __bf16 bf16x2 __attribute__((ext_vector_type(2)));
#define LAS __attribute__((address_space(3)))
#define GAS __attribute__((address_space(1)))

constexpr int SK = 32768, WIN = 4096, DH = 128, DM = 512, FF = 1792;
constexpr int WS_FO = 4096;
constexpr int WS_FY = WS_FO + 32768;
constexpr int WS_X = WS_FY + 8192;
constexpr int WS_Y = WS_X + 262144;
constexpr int WS_Q0 = WS_Y + 524288;
constexpr int WS_GC = WS_Q0 + 131072;
constexpr int WS_GRP = WS_GC + 65536, GRP_SZ = 143360, O_L = 61440;
constexpr int GO_ML = 0;
constexpr int GO_O = 2048;
constexpr int GO_G = 51200;
constexpr int GO_ST = 59392;
constexpr int GO_ST2 = O_L + 61440 + 8192;
constexpr int GO_Q1 = O_L + 61440;
static_assert(GO_ST2 + 128 <= GRP_SZ && 128 * 480 == O_L, "group block");
constexpr int WS_BYTES = WS_GRP + 8 * GRP_SZ;
constexpr int OOB = 0x7fffff00;
constexpr int WS_STAMP = 8 << 20;
constexpr int L_IMG = 0;
constexpr int L_IGU = 16384;
constexpr int L_IDN = 32768;
constexpr int L_IWO = 40960;
constexpr int L_STG = 57344, STG_W = 8704;
constexpr int L_ATT = L_STG + 4 * STG_W;
constexpr int L_ML = L_ATT + 8192;
constexpr int L_QB = L_ML + 128, QSTR = 320;
constexpr int L_XB = L_QB + 4 * QSTR;
constexpr int XB_SZ = 384;
constexpr int L_HB = L_XB + 3 * XB_SZ;
constexpr int L_FL = L_HB + 1024;
constexpr int L_RED = L_FL + 256, RED_ROW = 144, RED_SZ = 4 * RED_ROW;
constexpr int LDS_BYTES = 112 * 1024;
static_assert(L_RED + 2 * RED_SZ <= LDS_BYTES, "lds");
constexpr int TW = 3;
constexpr unsigned SPIN_CAP = 1u << 17;

constexpr int DL[16] = {  105,   290,   420,   555,   675,   825,   970,   1090,
                          3,   1220,   1,   400,   20,
                          15,   4,   40};

constexpr int NOM_G = 85, NOM_Q = 92, NOM_O = 120;
#define SCHED() __builtin_amdgcn_sched_barrier(0)
#define STAMP(i) do {} while (0)
#define PASS(i) do {} while (0)
#define STAMP_FLUSH() do {} while (0)
#define DELAY(i) do { if (DL[i] > 0) __builtin_amdgcn_s_sleep(DL[i]); } while (0)
#define DLV(i) DL[i]
__device__ __forceinline__ unsigned rtc() { return (unsigned)__builtin_amdgcn_s_memrealtime(); }
#define WAIT_AT(ref, i) do { SCHED(); unsigned tg_ = (ref) + (unsigned)DLV(i); const unsigned lo_ = rtc() + (unsigned)DLV(13); if ((int)(lo_ - tg_) > 0) tg_ = lo_; \
    for (int k_ = 0; k_ < 4096 && (int)(rtc() - tg_) < 0; ++k_) {} SCHED(); } while (0)
#define PW_A 0
#define VPRE 0
#define DN_LATE 1
#define PW_B 1
#define DST(k) do { if (DIAG == (k)) { SCHED(); *(volatile LAS unsigned*)((L + L_RED + 2 * RED_SZ + 64) + X.wv * 4) = rtc(); SCHED(); } } while (0)
#define UNLIKELY(x) __builtin_expect(!!(x), 0)
#define LIKELY(x) __builtin_expect(!!(x), 1)
#define PIN(v) asm volatile("" : "+v"(v))

__device__ __forceinline__ float bflo(unsigned u) { return __uint_as_float(u << 16); }
__device__ __forceinline__ float bfhi(unsigned u) { return __uint_as_float(u & 0xffff0000u); }
__device__ __forceinline__ unsigned pk(float lo, float hi) { bf16x2 p = {(__bf16)lo, (__bf16)hi}; return __builtin_bit_cast(unsigned, p); }
__device__ __forceinline__ float rbf(float f) { return bflo((unsigned)__builtin_bit_cast(u16, (__bf16)f)); }
__device__ __forceinline__ float vmax(float a, float b) { return __builtin_elementwise_maximum(a, b); }
template <int CTRL> __device__ __forceinline__ float dpp(float x) {
  return __builtin_bit_cast(float, __builtin_amdgcn_mov_dpp(__builtin_bit_cast(int, x), CTRL, 0xf, 0xf, true));
}
template <int CTRL> __device__ __forceinline__ unsigned dppu(unsigned x) { return (unsigned)__builtin_amdgcn_mov_dpp((int)x, CTRL, 0xf, 0xf, true); }
constexpr int XOR1 = 0xB1, XOR2 = 0x4E, HMIR = 0x141, MIR = 0x140, ROR4 = 0x124, ROR8 = 0x128;
__device__ __forceinline__ float row_sum(float v) { v += dpp<XOR1>(v); v += dpp<XOR2>(v); v += dpp<HMIR>(v); v += dpp<MIR>(v); return v; }
__device__ __forceinline__ float row_max(float v) { v = vmax(v, dpp<XOR1>(v)); v = vmax(v, dpp<XOR2>(v)); v = vmax(v, dpp<HMIR>(v)); v = vmax(v, dpp<MIR>(v)); return v; }
__device__ __forceinline__ float xrow_sum(float x) {
  auto s = __builtin_amdgcn_permlane16_swap(__float_as_uint(x), __float_as_uint(x), false, false);
  x = __uint_as_float(s[0]) + __uint_as_float(s[1]);
  auto t = __builtin_amdgcn_permlane32_swap(__float_as_uint(x), __float_as_uint(x), false, false);
  return __uint_as_float(t[0]) + __uint_as_float(t[1]);
}
__device__ __forceinline__ float xrow_max(float x) {
  auto s = __builtin_amdgcn_permlane16_swap(__float_as_uint(x), __float_as_uint(x), false, false);
  x = vmax(__uint_as_float(s[0]), __uint_as_float(s[1]));
  auto t = __builtin_amdgcn_permlane32_swap(__float_as_uint(x), __float_as_uint(x), false, false);
  return vmax(__uint_as_float(t[0]), __uint_as_float(t[1]));
}
__device__ __forceinline__ float rows_reduce4(f32x4 v) {
  auto a = __builtin_amdgcn_permlane16_swap(__float_as_uint(v[0]), __float_as_uint(v[1]), false, false);
  auto b = __builtin_amdgcn_permlane16_swap(__float_as_uint(v[2]), __float_as_uint(v[3]), false, false);
  const float t01 = __uint_as_float(a[0]) + __uint_as_float(a[1]), t23 = __uint_as_float(b[0]) + __uint_as_float(b[1]);
  auto c = __builtin_amdgcn_permlane32_swap(__float_as_uint(t01), __float_as_uint(t23), false, false);
  return __uint_as_float(c[0]) + __uint_as_float(c[1]);
}
__device__ __forceinline__ f32x4 mfma32(v4u a, v4u b, f32x4 c) {
  return __builtin_amdgcn_mfma_f32_16x16x32_bf16(__builtin_bit_cast(bf16x8, a), __builtin_bit_cast(bf16x8, b), c, 0, 0, 0);
}
__device__ __forceinline__ f32x4 mfma16(v2u a, v2u b, f32x4 c) {
  return __builtin_amdgcn_mfma_f32_16x16x16bf16_1k(__builtin_bit_cast(s16x4, a), __builtin_bit_cast(s16x4, b), c, 0, 0, 0);
}
__device__ __forceinline__ float dot2(unsigned a, unsigned b, float c) {
  return __builtin_amdgcn_fdot2_f32_bf16(__builtin_bit_cast(bf16x2, a), __builtin_bit_cast(bf16x2, b), c, false);
}
__device__ __forceinline__ v2u trread(LAS unsigned char* p) { return __builtin_bit_cast(v2u, __builtin_amdgcn_ds_read_tr16_b64_v4i16((LAS s16x4*)p)); }
__device__ __forceinline__ v4u ldg_nt(const u16* p) { return __builtin_nontemporal_load((const GAS v4u*)p); }
__device__ __forceinline__ v4u ld_sc1(__amdgpu_buffer_rsrc_t rs, int off) { return __builtin_amdgcn_raw_buffer_load_b128(rs, off, 0, 16); }
__device__ __forceinline__ unsigned ld1_sc1(__amdgpu_buffer_rsrc_t rs, int off) { return __builtin_amdgcn_raw_buffer_load_b32(rs, off, 0, 16); }
__device__ __forceinline__ v2u ld2_sc1(__amdgpu_buffer_rsrc_t rs, int off) { return __builtin_amdgcn_raw_buffer_load_b64(rs, off, 0, 16); }
__device__ __forceinline__ void st2_sc1(__amdgpu_buffer_rsrc_t rs, int off, v2u v) { __builtin_amdgcn_raw_buffer_store_b64(v, rs, off, 0, 16); }
__device__ __forceinline__ void st_sc1(__amdgpu_buffer_rsrc_t rs, int off, v4u v) { __builtin_amdgcn_raw_buffer_store_b128(v, rs, off, 0, 16); }
__device__ __forceinline__ void st1_sc1(__amdgpu_buffer_rsrc_t rs, int off, unsigned v) { __builtin_amdgcn_raw_buffer_store_b32(v, rs, off, 0, 16); }
#define PERM(hi, lo, sel) __builtin_amdgcn_perm((hi), (lo), (sel))
__device__ __forceinline__ bool okH(v4u d, unsigned T) { return (PERM(d.y, d.x, 0x05040100u) == T) & (PERM(d.w, d.z, 0x05040100u) == T); }
__device__ __forceinline__ v2u valH(v4u d) { return v2u{PERM(d.y, d.x, 0x07060302u), PERM(d.w, d.z, 0x07060302u)}; }
__device__ __forceinline__ v4u mkH(unsigned p0, unsigned p1, unsigned T) {
  return v4u{PERM(p0, T, 0x05040100u), PERM(p0, T, 0x07060302u), PERM(p1, T, 0x05040100u), PERM(p1, T, 0x07060302u)};
}
__device__ __forceinline__ v4u unF(v4u d, unsigned T) { return v4u{d.x ^ (T & 0xffu), d.y ^ ((T >> 8) & 0xffu), d.z ^ ((T >> 16) & 0xffu), d.w ^ (T >> 24)}; }
__device__ __forceinline__ unsigned or4(v4u d) { return (d.x | d.y) | (d.z | d.w); }
__device__ __forceinline__ v4u mkF(float a, float b, float c, float d, unsigned T) {
  return v4u{PERM(__float_as_uint(a), T, 0x07060500u), PERM(__float_as_uint(b), T, 0x07060501u),
             PERM(__float_as_uint(c), T, 0x07060502u), PERM(__float_as_uint(d), T, 0x07060503u)};
}
#define LGKM0() asm volatile("s_waitcnt lgkmcnt(0)" ::: "memory")
#define BARRIER() do { LGKM0(); __builtin_amdgcn_s_barrier(); asm volatile("" ::: "memory"); } while (0)

__device__ __forceinline__ float gemv128(v2u a0, v2u a1, const v2u (&B)[8]) {
  const f32x4 z = {0.f, 0.f, 0.f, 0.f};
  f32x4 c0 = mfma16(a0, B[0], z), c1 = mfma16(a0, B[1], z), c2 = mfma16(a0, B[2], z), c3 = mfma16(a0, B[3], z);
  c0 = mfma16(a1, B[4], c0); c1 = mfma16(a1, B[5], c1); c2 = mfma16(a1, B[6], c2); c3 = mfma16(a1, B[7], c3);
  return (c0[0] + c1[1]) + (c2[2] + c3[3]);
}
struct Ctx {
  const u16 *vc, *wo, *wg, *wu, *wd; u16* out; unsigned char* ws;
  __amdgpu_buffer_rsrc_t rs; LAS unsigned char* L;
  int lane, w, wv, kg, i16, b, g, c; unsigned E, T, t0, tref, dv;
};

#define LPOST(set) do { asm volatile("" ::: "memory"); *(volatile LAS unsigned*)(L + L_FL + (set) * 16 + w * 4) = T; asm volatile("" ::: "memory"); } while (0)
#define LWAIT4(set) do { for (unsigned spins_ = 0; spins_ < (1u << 24); ++spins_) { const v4u f_ = *(volatile LAS v4u*)(L + L_FL + (set) * 16); \
      if ((f_.x == T) & (f_.y == T) & (f_.z == T) & (f_.w == T)) break; } asm volatile("" ::: "memory"); } while (0)
#define LWAIT1(set, who) do { for (unsigned spins_ = 0; spins_ < (1u << 24); ++spins_) { \
      if (*(volatile LAS unsigned*)(L + L_FL + (set) * 16 + (who) * 4) == T) break; __builtin_amdgcn_s_sleep(1); } asm volatile("" ::: "memory"); } while (0)
#define ONES 0x3f803f80u
#define FL4(set) (*(volatile LAS v4u*)(L + L_FL + (set) * 16))
#define FL4OK(f) (((f).x == T) & ((f).y == T) & ((f).z == T) & ((f).w == T))

__device__ __forceinline__ v4u load_sw(const Ctx& X, int n) {
  const int cb = (X.c & 7) < 7 ? (X.c & 7) : 6;
  if (n < 4) return ldg_nt(X.wo + (size_t)(512 * X.g + 256 * (X.c & 1) + 64 * X.w + 16 * n + (X.lane >> 2)) * DM + 32 * (X.c >> 1) + 8 * (X.lane & 3));
  if (n < 8) return ldg_nt((n >= 6 ? X.wu : X.wg) + (size_t)(128 * (X.c >> 3) + 32 * X.w + 16 * (n & 1) + (X.lane >> 2)) * FF + 224 * X.g + 32 * cb + 8 * (X.lane & 3));
  const int blk = 2 * X.w + (n & 1) < 7 ? 2 * X.w + (n & 1) : 6;
  return ldg_nt(X.wd + (size_t)(224 * X.g + 32 * blk + (X.lane >> 1)) * DM + 16 * X.c + 8 * (X.lane & 1));
}
__device__ __forceinline__ void run_A(Ctx& X, v4u (&Kf)[8], v4u (&Vf)[8], v4u (&Sw)[10]) {
  const int lane = X.lane, w = X.w, kg = X.kg, i16 = X.i16, g = X.g, c = X.c;
  const __amdgpu_buffer_rsrc_t rs = X.rs; LAS unsigned char* L = X.L; LAS unsigned char* stg = L + L_STG + w * STG_W;
  const f32x4 z = {0.f, 0.f, 0.f, 0.f};
  STAMP(12); DST(12);
  const int u = (lane * 43) >> 7;
  const bool head3 = lane == 3 * u;
  int ooff = head3 ? WS_GRP + g * GRP_SZ + GO_O + w * 12288 + u * 512 + c * 16 : (lane == 1 ? WS_GRP + g * GRP_SZ + GO_ML + w * 512 + c * 16 : OOB);
  const int ostep = (head3 || lane == 1) ? O_L : 0;
  const u16* vb = X.vc + ((size_t)g * SK + (SK - WIN) + 128 * c + 32 * w) * DH;
  {
#pragma unroll
    for (int s = 0; s < 8; ++s) {
      *(LAS v4u*)(stg + (4 * s + kg) * 272 + i16 * 16) = Kf[s];
      SCHED();
      if (s + VPRE < 8) { const int j = s + VPRE; Vf[j] = ldg_nt(vb + (16 * (j >> 2) + 4 * kg + (j & 3)) * DH + 8 * i16); }
      SCHED();
    }
    LGKM0();
#pragma unroll
    for (int s = 0; s < 8; ++s) Kf[s] = *(LAS v4u*)(stg + (16 * (s >> 2) + i16) * 272 + (s & 3) * 64 + kg * 16);
  }
  {
    v4u t[8];
#pragma unroll
    for (int jj = 0; jj < 4; ++jj) {
#pragma unroll
      for (int n = 0; n < 8; ++n) t[n][jj] = PERM(Vf[2 * jj + 1][n >> 1], Vf[2 * jj][n >> 1], (n & 1) ? 0x07060302u : 0x05040100u);
      asm volatile("" : "+v"(t[0][jj]), "+v"(t[7][jj]));
      SCHED();
      Sw[jj] = load_sw(X, jj);
      SCHED();
    }
#pragma unroll
    for (int n = 0; n < 8; ++n) Vf[n] = t[n];
    asm volatile("" :: "v"(Vf[0]), "v"(Vf[1]), "v"(Vf[2]), "v"(Vf[3]), "v"(Vf[4]), "v"(Vf[5]), "v"(Vf[6]), "v"(Vf[7]));
  }
  STAMP(13); DST(10);
#pragma unroll
  for (int n = 0; n < 4; ++n) { *(LAS v4u*)(L + L_IWO + w * 4096 + n * 1024 + lane * 16) = Sw[n]; SCHED(); Sw[4 + n] = load_sw(X, 4 + n); SCHED(); }
#pragma unroll
  for (int n = 0; n < 4; ++n) {
    *(LAS v4u*)(L + L_IGU + (n >> 1) * 8192 + w * 2048 + (n & 1) * 1024 + lane * 16) = Sw[4 + n]; SCHED();
    if (n & 1) { Sw[8 + (n >> 1)] = load_sw(X, 8 + (n >> 1)); SCHED(); }
  }
  if (DN_LATE == 0) {
#pragma unroll
    for (int n = 0; n < 2; ++n) *(LAS v4u*)(L + L_IDN + (2 * w + n) * 1024 + lane * 16) = Sw[8 + n];
  }
  STAMP(9); DST(11);
#pragma unroll
  for (int layer = 0; layer < 2; ++layer) {
    BARRIER();
    const unsigned T = __builtin_amdgcn_readfirstlane(*(volatile LAS unsigned*)(L + L_FL));
    {
      v4u qf[4];
#pragma unroll
      for (int s = 0; s < 4; ++s) qf[s] = *(LAS v4u*)(L + L_QB + (i16 >> 2) * QSTR + s * 64 + kg * 16);
      f32x4 sa = z, sb = z;
#pragma unroll
      for (int s = 0; s < 4; ++s) { sa = mfma32(Kf[s], qf[s], sa); sb = mfma32(Kf[4 + s], qf[s], sb); }
      float sv[8];
      if (layer == 0) {
#pragma unroll
        for (int k = 0; k < 8; k += 2) {
          const unsigned p = k < 4 ? pk(sa[k & 3], sa[(k & 3) + 1]) : pk(sb[k & 3], sb[(k & 3) + 1]);
          const unsigned r = pk(bflo(p) * 0.08838834764831845f, bfhi(p) * 0.08838834764831845f);
          sv[k] = bflo(r); sv[k + 1] = bfhi(r);
        }
      } else {
#pragma unroll
        for (int k = 0; k < 4; ++k) { sv[k] = sa[k]; sv[4 + k] = sb[k]; }
      }
      const float CE = layer == 0 ? 1.4426950408889634f : 1.4426950408889634f * 0.08838834764831845f;
      const float m = xrow_max(vmax(vmax(vmax(sv[0], sv[1]), vmax(sv[2], sv[3])), vmax(vmax(sv[4], sv[5]), vmax(sv[6], sv[7])))) * CE;
#pragma unroll
      for (int k = 0; k < 8; ++k) sv[k] = __builtin_amdgcn_exp2f(fmaf(sv[k], CE, -m));
      const v4u pf = {pk(sv[0], sv[1]), pk(sv[2], sv[3]), pk(sv[4], sv[5]), pk(sv[6], sv[7])};
      f32x4 o[8];
#pragma unroll
      for (int n = 0; n < 8; ++n) o[n] = mfma32(pf, Vf[n], z);
      const f32x4 lv = mfma32(pf, v4u{ONES, ONES, ONES, ONES}, z);
      LAS unsigned char* op = L + L_ATT + w * 2048 + kg * 512 + i16 * 32;
      *(LAS f32x4*)op = f32x4{o[0][0], o[1][1], o[2][2], o[3][3]};
      *(LAS f32x4*)(op + 16) = f32x4{o[4][0], o[5][1], o[6][2], o[7][3]};
      if (i16 == 4 * kg) *(LAS f32x2*)(L + L_ML + kg * 32 + w * 8) = f32x2{m, lv[0]};
    }
    LPOST(1 + layer);
    const unsigned tm = rtc();
    {
      f32x4 ml0, ml1; f32x2 ov[4];
      for (unsigned sp = 0; sp < (1u << 24); ++sp) {
        const v4u f = FL4(1 + layer);
        ml0 = *(volatile LAS f32x4*)(L + L_ML + w * 32); ml1 = *(volatile LAS f32x4*)(L + L_ML + w * 32 + 16);
#pragma unroll
        for (int k = 0; k < 4; ++k) ov[k] = *(volatile LAS f32x2*)(L + L_ATT + k * 2048 + w * 512 + lane * 8);
        if (FL4OK(f)) break;
      }
      const float M = vmax(vmax(ml0[0], ml0[2]), vmax(ml1[0], ml1[2]));
      const float e0 = __builtin_amdgcn_exp2f(ml0[0] - M), e1 = __builtin_amdgcn_exp2f(ml0[2] - M), e2 = __builtin_amdgcn_exp2f(ml1[0] - M), e3 = __builtin_amdgcn_exp2f(ml1[2] - M);
      const f32x2 a = (ov[0] * e0 + ov[1] * e1) + (ov[2] * e2 + ov[3] * e3);
      const float ls = fmaf(e0, ml0[1], e1 * ml0[3]) + fmaf(e2, ml1[1], e3 * ml1[3]);
      unsigned dv = pk(a[0], a[1]);
      unsigned d1 = dppu<0x130>(dv);
      unsigned d2 = dppu<0x130>(d1);
      dv = head3 ? dv : __float_as_uint(M); d1 = head3 ? d1 : __float_as_uint(ls); d2 = head3 ? d2 : tm;
      st_sc1(rs, ooff, v4u{dv, d1, d2, T});
      ooff += ostep; asm volatile("" : "+v"(ooff));
    }
    if (layer == 0) { STAMP(3);
      if (DN_LATE) {
#pragma unroll
        for (int n = 0; n < 2; ++n) *(LAS v4u*)(L + L_IDN + (2 * w + n) * 1024 + lane * 16) = Sw[8 + n];
      }
      LPOST(15);
    } else STAMP(15);
  }
}

#define RETRY(pi, reload) if (LIKELY(__all(ok)) || spins > SPIN_CAP) break; asm volatile("" ::: "memory"); DELAY(8); PASS(pi); if (!ok) { reload; }
#define SLIP(i, lat) do { const int late_ = (int)(rtc() - (X.tref + (unsigned)DLV(i) + (unsigned)(lat))); if (late_ > 0) X.tref += (unsigned)(late_ + DLV(15)); } while (0)
#define RESYNC(tmax, i, nom) do { const int late_ = (int)((tmax) - (X.tref + (unsigned)DLV(i) + (unsigned)(nom))); if (late_ > 0) X.tref += (unsigned)late_; } while (0)
#define QUAD_TMAX(tv) ({ int dt_ = (int)((tv) - X.tref); dt_ = max(dt_, (int)dppu<XOR1>((unsigned)dt_)); dt_ = max(dt_, (int)dppu<XOR2>((unsigned)dt_)); X.tref + (unsigned)__builtin_amdgcn_readfirstlane(dt_); })
#define LN_STATS(ds) float s1 = __uint_as_float((ds).x), s2 = __uint_as_float((ds).y); \
      s1 += dpp<XOR1>(s1); s2 += dpp<XOR1>(s2); s1 += dpp<XOR2>(s1); s2 += dpp<XOR2>(s2); \
      const float mean = s1 * (1.f / DM), rstd = __builtin_amdgcn_rsqf(fmaxf(fmaf(-mean, mean, s2 * (1.f / DM)), 0.f) + 1e-5f)

template <int layer>
__device__ __forceinline__ void run_B(Ctx& X, const v2u (&Bq)[8], v4u (&Aw)[12], v2u (&Bg)[8], v4u (&Bd)[7], float& xr, f32x4& csq, float (&csg)[2]) {
  const int lane = X.lane, w = X.w, kg = X.kg, i16 = X.i16, g = X.g, c = X.c, kq = c >> 3;
  const unsigned T = X.T; const __amdgpu_buffer_rsrc_t rs = X.rs; LAS unsigned char* L = X.L;
  LAS unsigned char* red = L + L_RED + layer * RED_SZ;
  const f32x4 z = {0.f, 0.f, 0.f, 0.f};
  const int grp = WS_GRP + g * GRP_SZ;
  v2u fy0; v4u c0, c1; unsigned tq;
  const int qun = kg * 8 + (lane & 7), qidx = ((lane >> 3) & 1) * 2048 + (32 * w + qun) * 16;
  const int xun = 8 * w + (lane & 7);
  {
    int qoff = (layer == 0 ? WS_Q0 + g * 8192 : grp + GO_Q1) + qidx, soff = grp + GO_ST2 + (lane & 3) * 16;
    PIN(qoff); PIN(soff);
    if (layer == 0) WAIT_AT(X.t0, 11); else WAIT_AT(X.tref, 4);
    v4u ds = v4u{0u, 0u, 0u, T}, r0, r1;
#define QLOADS() do { if (layer == 1) ds = ld_sc1(rs, soff); r0 = ld_sc1(rs, qoff); r1 = ld_sc1(rs, qoff + 4096); } while (0)
    QLOADS();
    SCHED();
    for (unsigned spins = 0;; ++spins) {
      LN_STATS(ds);
      const v4u d0 = unF(r0, T), d1 = unF(r1, T);
      float qv[4];
#pragma unroll
      for (int k = 0; k < 4; ++k) {
        float t = __uint_as_float(d0[k]) + __uint_as_float(d1[k]); t += dpp<ROR8>(t);
        qv[k] = layer == 0 ? t : rstd * fmaf(-mean, csq[k], t);
      }
      *(LAS v2u*)(L + L_QB + w * QSTR + qun * 8) = v2u{pk(qv[0], qv[1]), pk(qv[2], qv[3])};
      const bool ok = (((or4(d0) | or4(d1)) & 0xffu) == 0u) & (ds.w == T);
      RETRY(layer ? 7 : 0, QLOADS());
    }
    tq = ds.z;
    STAMP(2 + 12 * layer); if (layer == 0) DST(2);
  }
  BARRIER();
  if (layer == 1) { RESYNC(QUAD_TMAX(tq), 3, NOM_Q); SLIP(4, 72); }
  if (layer == 0) {
    tq = rtc();
    if (w == TW && lane == 0) st2_sc1(rs, WS_FO + (g * 32 + c) * 128, v2u{T, tq});
    if (w == TW) { const int base_ = (DIAG >= 10) ? 4 : 0; int m_ = -(1 << 30); for (int k = 0; k < 4; ++k) m_ = max(m_, (int)(*(volatile LAS unsigned*)((L + L_RED + 2 * RED_SZ + 64) + (base_ + k) * 4) - X.t0));
      if (lane == 0) st_sc1(rs, WS_FO + (g * 32 + c) * 128 + 64, v4u{T, X.t0, X.t0 + (unsigned)m_, 0u}); }
    fy0 = ld2_sc1(rs, w == TW ? WS_FO + (g * 32 + (lane & 31)) * 128 : OOB);
    c0 = ld_sc1(rs, WS_Q0 + 65536 + g * 8192 + qidx); c1 = ld_sc1(rs, WS_Q0 + 65536 + g * 8192 + qidx + 4096);
    SCHED();
  }
#define WO_PREP() do { const int hh = w >> 1; \
_Pragma("unroll") \
    for (int t = 0; t < 6; ++t) { \
      const int un = 4 * t + kg, r0 = 6 * un + (i16 >> 2); \
      const int ra = r0 > 127 ? 127 : r0, rb = r0 + 4 > 127 ? 127 : r0 + 4; \
_Pragma("unroll") \
      for (int ct = 0; ct < 2; ++ct) { \
        const v2u lo = trread(L + L_IWO + (hh * 128 + ra) * 64 + ct * 32 + (i16 & 3) * 8), hi = trread(L + L_IWO + (hh * 128 + rb) * 64 + ct * 32 + (i16 & 3) * 8); \
        Aw[t * 2 + ct] = v4u{(t == 5 && un > 21) ? 0u : lo.x, (t == 5 && un > 20) ? 0u : lo.y, (t == 5 && un > 20) ? 0u : hi.x, 0u}; \
      } \
    } \
  } while (0)
  if (layer == 0) {
    WO_PREP();
#pragma unroll
    for (int u = 0; u < 8; ++u) Bg[u] = trread(L + L_IGU + (w >> 1) * 8192 + (64 * (u >> 2) + 16 * kg + 4 * (u & 3) + (i16 >> 2)) * 64 + (w & 1) * 32 + (i16 & 3) * 8);
    const v2u one = {ONES, ONES};
    const float cs = gemv128(one, one, Bg);
    const float p1 = dpp<0x55>(cs), p2 = dpp<0xAA>(cs), p3 = dpp<0xFF>(cs);
    st_sc1(rs, ((lane & 0x33) == 0 && (c & 7) < 7) ? WS_GC + g * 8192 + kq * 2048 + (w >> 1) * 1024 + (8 * (c & 7) + 4 * (w & 1) + (i16 >> 2)) * 16 : OOB, mkF(cs, p1, p2, p3, T));

  }
  {
    const int hh = w >> 1, st = w & 1, h = 2 * (c & 1) + hh;
    int obase = grp + GO_O + layer * O_L + h * 12288 + (16 * st + i16) * 16 + kg * 512;
    int o5 = obase + 5 * 2048 - (kg & 2) * 512;
    const int rot = (4 * kg + i16) & 15;
    int mlA = grp + GO_ML + layer * O_L + h * 512 + (16 * st + rot) * 16, mlB = mlA ^ 256;
    const int slot = ((lane >> 4) & 1) * 4 + (lane & 3), rep = ((lane >> 2) & 3) + 4 * (lane >> 5);
    int yst = WS_Y + layer * 262144 + rep * 32768 + (2 * g + (c & 1)) * 2048 + (c >> 1) * 128 + slot * 16;
    PIN(obase); PIN(o5); PIN(mlA); PIN(mlB); PIN(yst);
    if (layer == 0) {
      unsigned tg;
      if (w == TW) {
        for (unsigned spins = 0;; ++spins) {
          if ((lane & 31) == c) fy0 = v2u{T, tq};
          if (__all(fy0.x == T) || spins > SPIN_CAP) break;
          asm volatile("" ::: "memory"); DELAY(10); PASS(1);
          fy0 = ld2_sc1(rs, WS_FO + (g * 32 + (lane & 31)) * 128);
        }
        int dt = (int)(fy0.y - X.t0); dt = max(dt, (int)dppu<XOR1>((unsigned)dt)); dt = max(dt, (int)dppu<XOR2>((unsigned)dt)); dt = max(dt, (int)dppu<HMIR>((unsigned)dt));
        dt = max(dt, (int)dppu<MIR>((unsigned)dt)); dt = max(__builtin_amdgcn_readlane(dt, 0), __builtin_amdgcn_readlane(dt, 16)); tg = X.t0 + (unsigned)dt;
        *(volatile LAS unsigned*)(L + L_FL + 3 * 16) = tg; LPOST(3);
      } else { LWAIT1(3, TW); tg = __builtin_amdgcn_readfirstlane(*(volatile LAS unsigned*)(L + L_FL + 3 * 16)); }
      if (c == 0 && w == TW && lane < 8) st2_sc1(rs, WS_FY + (lane * 8 + g) * 128, v2u{T, tg});
      for (unsigned spins = 0;; ++spins) {
        c0 = unF(c0, T); c1 = unF(c1, T);
        if (__all(((or4(c0) | or4(c1)) & 0xffu) == 0u) || spins > SPIN_CAP) break;
        asm volatile("" ::: "memory"); DELAY(8); c0 = ld_sc1(rs, WS_Q0 + 65536 + g * 8192 + qidx); c1 = ld_sc1(rs, WS_Q0 + 65536 + g * 8192 + qidx + 4096);
      }
#pragma unroll
      for (int k = 0; k < 4; ++k) { float t = __uint_as_float(c0[k]) + __uint_as_float(c1[k]); t += dpp<ROR8>(t); csq[k] = t; }
      { unsigned t_ = tg + (unsigned)DLV(0); for (int k_ = 0; k_ < 4096 && (int)(rtc() - t_) < 0; ++k_) {} }
      X.tref = tg;
    } else WAIT_AT(X.tref, 5);
    if (layer == 0) fy0 = ld2_sc1(rs, WS_FY + (g * 8 + (lane & 7)) * 128);
    v4u d[8];
#define OLOADS() do { d[6] = ld_sc1(rs, mlA); d[7] = ld_sc1(rs, mlB); _Pragma("unroll") for (int t = 0; t < 5; ++t) d[t] = ld_sc1(rs, obase + t * 2048); d[5] = ld_sc1(rs, o5); } while (0)
    OLOADS();
    SCHED();
    for (unsigned spins = 0;; ++spins) {
      const float mA = __uint_as_float(d[6].x), lA = __uint_as_float(d[6].y), mB = __uint_as_float(d[7].x), lB = __uint_as_float(d[7].y);
      const float M = row_max(vmax(mA, mB));
      const float eA = __builtin_amdgcn_exp2f(mA - M), eB = __builtin_amdgcn_exp2f(mB - M);
      const float Ls = row_sum(fmaf(eA, lA, eB * lB));
      const float wgt = eA * __builtin_amdgcn_rcpf(Ls);
      const float w0 = dpp<0x150>(wgt), w1 = dpp<0x151>(wgt), w2 = dpp<0x152>(wgt), w3 = dpp<0x153>(wgt);
      f32x4 a0 = z, a1 = z;
#pragma unroll
      for (int t = 0; t < 6; ++t) { a0 = mfma32(d[t], Aw[2 * t], a0); a1 = mfma32(d[t], Aw[2 * t + 1], a1); }
      const float y0 = fmaf(a0[0], w0, a0[1] * w1) + fmaf(a0[2], w2, a0[3] * w3), y1 = fmaf(a1[0], w0, a1[1] * w1) + fmaf(a1[2], w2, a1[3] * w3);
      auto sw = __builtin_amdgcn_permlane16_swap(__float_as_uint(y0), __float_as_uint(y1), false, false);
      const float yy = __uint_as_float(sw[0]) + __uint_as_float(sw[1]);
      auto sx = __builtin_amdgcn_permlane32_swap(__float_as_uint(yy), __float_as_uint(yy), false, false);
      *(LAS float*)(red + w * RED_ROW + (lane & 31) * 4) = __uint_as_float(sx[0]) + __uint_as_float(sx[1]);
      const bool ok = (d[0].w == T) & (d[1].w == T) & (d[2].w == T) & (d[3].w == T) & (d[4].w == T) & (d[5].w == T) & (d[6].w == T) & (d[7].w == T);
      RETRY(layer ? 8 : 1, OLOADS());
    }
    if (layer == 0) asm volatile("s_waitcnt vmcnt(0)" ::: "memory");
    LPOST(4 + 5 * layer);
    STAMP(4 + 12 * layer);
    if (w == TW) {
      f32x4 v;
      for (unsigned sp = 0; sp < (1u << 24); ++sp) {
        const v4u f = FL4(4 + 5 * layer);
        v = *(volatile LAS f32x4*)(red + ((lane >> 2) & 3) * RED_ROW + slot * 16);
        if (FL4OK(f)) break;
      }
#pragma unroll
      for (int k = 0; k < 4; ++k) { float t = v[k]; t += dpp<ROR8>(t); t += dpp<ROR4>(t); v[k] = t; }
      st_sc1(rs, yst, mkF(v[0], v[1], v[2], v[3], T));
    }
    if (layer == 1) {
      int dt = max((int)(d[6].z - X.tref), (int)(d[7].z - X.tref));
      dt = max(dt, (int)dppu<XOR1>((unsigned)dt)); dt = max(dt, (int)dppu<XOR2>((unsigned)dt)); dt = max(dt, (int)dppu<HMIR>((unsigned)dt)); dt = max(dt, (int)dppu<MIR>((unsigned)dt));
      RESYNC(X.tref + (unsigned)__builtin_amdgcn_readfirstlane(dt), 4, NOM_O);
      SLIP(5, 125);
    }
  }
  STAMP(5 + 12 * layer);
#define X_SHARE(xb, setx, sets) do { LPOST(setx); \
    float s1 = xr, s2 = xr * xr; \
    s1 += dpp<XOR1>(s1); s2 += dpp<XOR1>(s2); s1 += dpp<XOR2>(s1); s2 += dpp<XOR2>(s2); s1 += dpp<HMIR>(s1); s2 += dpp<HMIR>(s2); \
    *(LAS f32x2*)((xb) + 256 + (4 * w + kg) * 8) = f32x2{s1, s2}; LPOST(sets); } while (0)
#define X_FETCH(xb, setx) v2u a0, a1; for (unsigned sp = 0; sp < (1u << 24); ++sp) { const v4u f = FL4(setx); \
      a0 = *(volatile LAS v2u*)((xb) + (4 * kg + (i16 & 3)) * 8); a1 = *(volatile LAS v2u*)((xb) + 128 + (4 * kg + (i16 & 3)) * 8); if (FL4OK(f)) break; }
#define STATS_PUBLISH(xb, sets, idx) do { f32x2 sp2; for (unsigned sp = 0; sp < (1u << 24); ++sp) { const v4u f = FL4(sets); sp2 = *(volatile LAS f32x2*)((xb) + 256 + i16 * 8); if (FL4OK(f)) break; } \
    const float S1 = row_sum(sp2[0]), S2 = row_sum(sp2[1]); \
    st_sc1(rs, lane == 0 ? grp + (idx) + kq * 16 : OOB, v4u{__float_as_uint(S1), __float_as_uint(S2), rtc(), T}); } while (0)
  {
    const int go = kq * 2048 + (w >> 1) * 1024 + (8 * (c & 7) + 4 * (w & 1) + (i16 >> 2)) * 16;
    const bool gst = (lane & 0x33) == 0 && (c & 7) < 7;
    int goff = gst ? grp + GO_G + layer * O_L + go : OOB;
    int yoff = WS_Y + layer * 262144 + g * 32768 + (lane >> 3) * 2048 + (32 * kq + xun) * 16;
    PIN(goff); PIN(yoff);
    if (layer == 0) {
      {
        for (unsigned spins = 0;; ++spins) {
          if ((lane & 7) == g) fy0 = v2u{T, X.tref};
          if (__all(fy0.x == T) || spins > SPIN_CAP) break;
          asm volatile("" ::: "memory"); DELAY(14); PASS(3);
          fy0 = ld2_sc1(rs, WS_FY + (g * 8 + (lane & 7)) * 128);
        }
        int dt = (int)(fy0.y - X.t0); dt = max(dt, (int)dppu<XOR1>((unsigned)dt)); dt = max(dt, (int)dppu<XOR2>((unsigned)dt)); dt = max(dt, (int)dppu<HMIR>((unsigned)dt));
        X.tref = X.t0 + (unsigned)__builtin_amdgcn_readfirstlane(dt);
      }
      { unsigned t_ = X.tref + (unsigned)DLV(1); for (int k_ = 0; k_ < 4096 && (int)(rtc() - t_) < 0; ++k_) {} }
    } else WAIT_AT(X.tref, 6);
    LAS unsigned char* xb = L + L_XB + 2 * layer * XB_SZ;
    float xn;
    v4u r0 = ld_sc1(rs, yoff), r1 = ld_sc1(rs, yoff + 16384);
    SCHED();
    for (unsigned spins = 0;; ++spins) {
      const v4u d0 = unF(r0, T), d1 = unF(r1, T);
      float t = rows_reduce4(__builtin_bit_cast(f32x4, d0) + __builtin_bit_cast(f32x4, d1));
      t += dpp<ROR8>(t);
      const unsigned p = pk(xr + t, 0.f);
      *(LAS u16*)(xb + (4 * xun + kg) * 2) = (u16)p;
      xn = bflo(p);
      const bool ok = ((or4(d0) | or4(d1)) & 0xffu) == 0u;
      RETRY(layer ? 9 : 4, r0 = ld_sc1(rs, yoff); r1 = ld_sc1(rs, yoff + 16384));
    }
    xr = xn;
    STAMP(6 + 12 * layer);
    X_SHARE(xb, 6 + 4 * layer, 12 + layer);
    if ((c & 7) < 7) {
      X_FETCH(xb, 6 + 4 * layer);
      const float part = gemv128(a0, a1, Bg);
      const float p1 = dpp<0x55>(part), p2 = dpp<0xAA>(part), p3 = dpp<0xFF>(part);
      st_sc1(rs, goff, mkF(part, p1, p2, p3, T));
    } else if (w == 0) STATS_PUBLISH(xb, 12 + layer, GO_ST + layer * O_L);
  }
  if (layer == 1) STAMP(19);
  SLIP(layer == 0 ? 1 : 6, 98);
  if (layer == 0) {
    const v4u zz = {0u, 0u, 0u, 0u};
    const int slot = ((lane >> 4) & 1) * 4 + (lane & 3), rep = ((lane >> 2) & 3) + 4 * (lane >> 5);
    if (w == TW) {
      st_sc1(rs, WS_Y + 262144 + rep * 32768 + (2 * g + (c & 1)) * 2048 + (c >> 1) * 128 + slot * 16, zz);
      st_sc1(rs, i16 < 8 ? WS_X + 131072 + i16 * 16384 + g * 2048 + c * 64 + kg * 16 : OOB, zz);
    }
    st_sc1(rs, lane < 30 ? grp + O_L + (c * 4 + w) * 480 + lane * 16
                         : ((lane & 0x33) == 0x20 ? grp + GO_Q1 + kq * 2048 + ((c & 7) * 64 + w * 16 + i16) * 4 : ((lane == 63 && (c & 7) == 0 && w == 0) ? grp + GO_ST2 + kq * 16 : OOB)), zz);
  }
  {
    const int hu = 14 * w + (i16 < 14 ? i16 : 13);
    const int gi = kg * 2048 + hu * 16;
    int gld = grp + GO_G + layer * O_L + gi, soff = grp + GO_ST + layer * O_L + (lane & 3) * 16;
    int xst = i16 < 8 ? WS_X + layer * 131072 + i16 * 16384 + g * 2048 + c * 64 + kg * 16 : OOB;
    PIN(gld); PIN(soff); PIN(xst);
    if (layer == 0 && w == TW) {
      LWAIT4(15);
#pragma unroll
      for (int t = 0; t < 7; ++t) {
        const int r0 = 32 * t + 8 * kg + (i16 >> 2);
        const v2u lo = trread(L + L_IDN + r0 * 32 + (i16 & 3) * 8), hi = trread(L + L_IDN + (r0 + 4) * 32 + (i16 & 3) * 8);
        Bd[t] = v4u{lo.x, lo.y, hi.x, hi.y};
      }
    }
    WAIT_AT(X.tref, layer == 0 ? 2 : 7);
    LAS unsigned char* hb = L + L_HB + layer * 512;
    v4u ds, r0, r1;
#define GLOADS() do { ds = ld_sc1(rs, soff); if (layer == 0) { c0 = ld_sc1(rs, WS_GC + g * 8192 + gi); c1 = ld_sc1(rs, WS_GC + g * 8192 + gi + 1024); } \
      r0 = ld_sc1(rs, gld); r1 = ld_sc1(rs, gld + 1024); } while (0)
    GLOADS();
    SCHED();
    for (unsigned spins = 0;; ++spins) {
      LN_STATS(ds);
      unsigned cbad = 0u;
      if (layer == 0) {
        const v4u e0 = unF(c0, T), e1 = unF(c1, T); cbad = or4(e0) | or4(e1);
        csg[0] = rows_reduce4(__builtin_bit_cast(f32x4, e0)); csg[1] = rows_reduce4(__builtin_bit_cast(f32x4, e1));
      }
      const v4u d0 = unF(r0, T), d1 = unF(r1, T);
      const float gv = rstd * fmaf(-mean, csg[0], rows_reduce4(__builtin_bit_cast(f32x4, d0)));
      const float uv = rstd * fmaf(-mean, csg[1], rows_reduce4(__builtin_bit_cast(f32x4, d1)));
      const float hv = uv * __builtin_amdgcn_rcpf(1.f + __builtin_amdgcn_exp2f(-1.4426950408889634f * gv));
      *(LAS u16*)(hb + (4 * hu + kg) * 2) = (u16)pk(hv, 0.f);
      const bool ok = (((or4(d0) | or4(d1) | cbad) & 0xffu) == 0u) & (ds.w == T);
      RETRY(5, GLOADS());
    }
    if (layer == 0) asm volatile("s_waitcnt vmcnt(0)" ::: "memory");
    LPOST(7 + 4 * layer);
    STAMP(8 + 12 * layer);
    if (w == TW) {
      v4u hf[7];
      for (unsigned sp = 0; sp < (1u << 24); ++sp) {
        const v4u f = FL4(7 + 4 * layer);
#pragma unroll
        for (int t = 0; t < 7; ++t) hf[t] = *(volatile LAS v4u*)(hb + t * 64 + kg * 16);
        if (FL4OK(f)) break;
      }
      f32x4 a0 = z, a1 = z;
#pragma unroll
      for (int t = 0; t < 7; ++t) { if (t & 1) a1 = mfma32(Bd[t], hf[t], a1); else a0 = mfma32(Bd[t], hf[t], a0); }
      a0 += a1;
      st_sc1(rs, xst, mkF(a0[0], a0[1], a0[2], a0[3], T));
    }
    if (layer == 0) STAMP(10);
    RESYNC(QUAD_TMAX(ds.z), layer == 0 ? 1 : 6, NOM_G);
    SLIP(layer == 0 ? 2 : 7, layer == 0 ? 125 : 105);
  }
  if (layer == 0 || (g == 0 && (c & 7) == 0)) {
    int xoff = WS_X + layer * 131072 + g * 16384 + (lane >> 3) * 2048 + (32 * kq + xun) * 16;
    int qst = (lane & 0x33) == 0 ? grp + GO_Q1 + kq * 2048 + ((c & 7) * 64 + w * 16 + i16) * 4 : OOB;
    PIN(xoff); PIN(qst);
    WAIT_AT(X.tref, layer == 0 ? 3 : 9);
    LAS unsigned char* xb = L + L_XB + XB_SZ;
    unsigned p;
    v4u r0 = ld_sc1(rs, xoff);
    for (unsigned spins = 0;; ++spins) {
      const v4u d0 = unF(r0, T);
      float t = rows_reduce4(__builtin_bit_cast(f32x4, d0));
      t += dpp<ROR8>(t);
      p = pk(xr + t, 0.f);
      if (layer == 0) *(LAS u16*)(xb + (4 * xun + kg) * 2) = (u16)p;
      const bool ok = (or4(d0) & 0xffu) == 0u;
      RETRY(6, r0 = ld_sc1(rs, xoff));
    }
    if (layer == 0) {
      xr = bflo(p);
      X_SHARE(xb, 8, 14);
      X_FETCH(xb, 8);
      const float part = gemv128(a0, a1, Bq);
      const float p1 = dpp<0x55>(part), p2 = dpp<0xAA>(part), p3 = dpp<0xFF>(part);
      st_sc1(rs, qst, mkF(part, p1, p2, p3, T));
      if ((c & 7) == 0 && w == 0) STATS_PUBLISH(xb, 14, GO_ST2);
      STAMP(11);
      SLIP(3, 100);
    } else {
      if ((lane & 8) == 0) *(GAS u16*)(X.out + 128 * kq + 4 * xun + kg) = (u16)p;
      if (c == 0 && w == 0 && lane == 0) st1_sc1(rs, 0, X.E + 1u);
      if (c == 0 && w == 0) {
        int mn = 1 << 30, mx = -(1 << 30);
        for (int k = 0; k < 4; ++k) { const v4u f = ld_sc1(rs, WS_FO + (k * 64 + lane) * 128 + 64); mn = min(mn, (int)(f.y - X.t0)); mx = max(mx, (int)(f.z - X.t0)); }
        for (int o = 32; o >= 1; o >>= 1) { mn = min(mn, __shfl_xor(mn, o)); mx = max(mx, __shfl_xor(mx, o)); }
        const int dv = __builtin_amdgcn_readfirstlane(DIAG == 0 ? 0 : mx - mn);
        const unsigned e_ = rtc() + (unsigned)dv; for (int k_ = 0; k_ < 100000 && (int)(rtc() - e_) < 0; ++k_) {}
      }
      STAMP(21);
    }
  }
}

__global__ void __launch_bounds__(512, 2)
fused_decode(const u16* x, const u16* kc, const u16* vc, const u16* wq, const u16* wo, const u16* wg, const u16* wu, const u16* wd, u16* out, unsigned char* ws) {
  extern __shared__ __attribute__((aligned(16))) unsigned char smem[];
  Ctx X;
  const int tid = threadIdx.x;
  X.lane = tid & 63; X.wv = __builtin_amdgcn_readfirstlane(tid >> 6); X.w = X.wv & 3; X.kg = X.lane >> 4; X.i16 = X.lane & 15;
  X.b = blockIdx.x; X.g = X.b & 7; X.c = X.b >> 3;
  X.t0 = rtc();
  unsigned pfv;
  {
    unsigned long long pc; unsigned len;
    asm volatile("s_getpc_b64 %0\n\tLpf_here:\n\ts_mov_b32 %1, Lpf_end - Lpf_here" : "=s"(pc), "=s"(len));
    unsigned off = (unsigned)(X.c * 8 + X.wv) * 128u; off = off < len ? off : 0u;
    pfv = *(const GAS unsigned*)(pc + off);
  }
  SCHED();
  asm volatile("" : "+s"(x), "+s"(kc), "+s"(vc), "+s"(wq), "+s"(wo), "+s"(wg), "+s"(wu), "+s"(wd), "+s"(out), "+s"(ws));
  SCHED();
  X.vc = vc; X.wo = wo; X.wg = wg; X.wu = wu; X.wd = wd; X.out = out; X.ws = ws;
  X.L = (LAS unsigned char*)smem;
  X.rs = __builtin_amdgcn_make_buffer_rsrc((void*)ws, 0, WS_BYTES, 0x00020000);
  const int lane = X.lane, w = X.w, kg = X.kg, i16 = X.i16, g = X.g, c = X.c, kq = c >> 3;
  const __amdgpu_buffer_rsrc_t rs = X.rs; LAS unsigned char* L = X.L;
  v4u Kf[8], Vf[8], Sw[10], wr[4], xf; v2u xa, xb; unsigned xu, E;
  if (X.wv >= 4) {
    DELAY(12);
    const size_t kvbase = ((size_t)g * SK + (SK - WIN) + 128 * c + 32 * w) * DH;
#pragma unroll
    for (int s = 0; s < 8; ++s) Kf[s] = ldg_nt(kc + kvbase + s * 512 + lane * 8);
#pragma unroll
    for (int j = 0; j < VPRE; ++j) Vf[j] = ldg_nt(vc + kvbase + (16 * (j >> 2) + 4 * kg + (j & 3)) * DH + 8 * i16);
  } else {
#pragma unroll
    for (int n = 0; n < 4; ++n) wr[n] = ldg_nt(wq + (size_t)(128 * kq + 32 * w + 8 * n + (lane >> 3)) * 4096 + 512 * g + 64 * (c & 7) + 8 * (lane & 7));
    xf = *(const GAS v4u*)(x + 8 * lane);
    xa = *(const GAS v2u*)(x + 4 * (32 * kq + 4 * kg + (i16 & 3))); xb = *(const GAS v2u*)(x + 4 * (32 * kq + 16 + 4 * kg + (i16 & 3)));
    xu = *(const GAS u16*)(x + 128 * kq + 4 * (8 * w + (lane & 7)) + kg);
    E = ld1_sc1(rs, 0);
    SCHED();
    {
      const v4u zz = {0u, 0u, 0u, 0u};
      const int slot = ((lane >> 4) & 1) * 4 + (lane & 3), rep = ((lane >> 2) & 3) + 4 * (lane >> 5), grp = WS_GRP + g * GRP_SZ;
      if (w == TW) {
        st_sc1(rs, WS_Y + rep * 32768 + (2 * g + (c & 1)) * 2048 + (c >> 1) * 128 + slot * 16, zz);
        st_sc1(rs, i16 < 8 ? WS_X + i16 * 16384 + g * 2048 + c * 64 + kg * 16 : OOB, zz);
      }
      st_sc1(rs, (c & 7) < 7 ? ((lane & 0x33) == 0 ? grp + GO_G + kq * 2048 + (w >> 1) * 1024 + (8 * (c & 7) + 4 * (w & 1) + (i16 >> 2)) * 16 : OOB)
                             : ((w == 0 && lane == 0) ? grp + GO_ST + kq * 16 : OOB), zz);
    }
  }
  SCHED();
  int wv2 = X.wv; asm volatile("" : "+s"(wv2));
  if (wv2 >= 4) run_A(X, Kf, Vf, Sw);
  int wv3 = X.wv; asm volatile("" : "+s"(wv3));
  if (wv3 < 4) {
#pragma unroll
    for (int n = 0; n < 4; ++n) *(LAS v4u*)(L + L_IMG + w * 4096 + n * 1024 + lane * 16) = wr[n];
    const unsigned hsh = (E + 1u) * 0x9E3779B1u;
    const unsigned T = __builtin_amdgcn_readfirstlane(((hsh & 0xbfffbf00u) | (((hsh >> 8) & 0xbfu) ^ 0xa5u)));
    X.E = E; X.T = T;
    LPOST(0); DST(3);
    float xr = bflo(xu);
    v2u Bq[8], Bg[8]; v4u Aw[12], Bd[7];
    {
      float xv[8] = {bflo(xf.x), bfhi(xf.x), bflo(xf.y), bfhi(xf.y), bflo(xf.z), bfhi(xf.z), bflo(xf.w), bfhi(xf.w)};
      float s = 0.f;
#pragma unroll
      for (int k = 0; k < 8; ++k) s += xv[k];
      const float mean = xrow_sum(row_sum(s)) * (1.f / DM);
      float v = 0.f;
#pragma unroll
      for (int k = 0; k < 8; ++k) { const float d = xv[k] - mean; v = fmaf(d, d, v); }
      const float var = xrow_sum(row_sum(v)) * (1.f / DM);
      const float mb = rbf(mean), isd = 1.0f / rbf(sqrtf(rbf(rbf(var) + 1e-5f)));
      const float xi[8] = {bflo(xa.x), bfhi(xa.x), bflo(xa.y), bfhi(xa.y), bflo(xb.x), bfhi(xb.x), bflo(xb.y), bfhi(xb.y)};
      float xn[8];
#pragma unroll
      for (int k = 0; k < 8; ++k) xn[k] = rbf(xi[k] - mb) * isd;
      const v2u a0 = {pk(xn[0], xn[1]), pk(xn[2], xn[3])}, a1 = {pk(xn[4], xn[5]), pk(xn[6], xn[7])};
      LWAIT4(0);
#pragma unroll
      for (int u = 0; u < 8; ++u) Bq[u] = trread(L + L_IMG + (64 * (u >> 2) + 16 * kg + 4 * (u & 3) + (i16 >> 2)) * 128 + w * 32 + (i16 & 3) * 8);
      const int qo = (lane & 0x33) == 0 ? WS_Q0 + g * 8192 + kq * 2048 + ((c & 7) * 64 + w * 16 + i16) * 4 : OOB - 65536;
      {
        const float part = gemv128(a0, a1, Bq);
        const float p1 = dpp<0x55>(part), p2 = dpp<0xAA>(part), p3 = dpp<0xFF>(part);
        st_sc1(rs, qo, mkF(part, p1, p2, p3, T));
      }
      STAMP(1); DST(1);
      {
        const v2u one = {ONES, ONES};
        const float part = gemv128(one, one, Bq);
        const float p1 = dpp<0x55>(part), p2 = dpp<0xAA>(part), p3 = dpp<0xFF>(part);
        st_sc1(rs, qo + 65536, mkF(part, p1, p2, p3, T));
      }
    }
    f32x4 csq = {0.f, 0.f, 0.f, 0.f}; float csg[2] = {0.f, 0.f};
    run_B<0>(X, Bq, Aw, Bg, Bd, xr, csq, csg);
    run_B<1>(X, Bq, Aw, Bg, Bd, xr, csq, csg);
  }
  asm volatile("" :: "v"(pfv));
  STAMP_FLUSH();
  asm volatile("Lpf_end:");
}

extern "C" void kernel_launch(void* const* d_in, const int* in_sizes, int n_in, void* d_out, int out_size, void* d_ws, size_t ws_size,
                              hipStream_t stream) {
  (void)in_sizes; (void)n_in; (void)out_size; (void)ws_size;
  (void)hipFuncSetAttribute((const void*)fused_decode, hipFuncAttributeMaxDynamicSharedMemorySize, LDS_BYTES);
  hipLaunchKernelGGL(fused_decode, dim3(256), dim3(512), LDS_BYTES, stream, (const u16*)d_in[0], (const u16*)d_in[1], (const u16*)d_in[2],
                     (const u16*)d_in[3], (const u16*)d_in[4], (const u16*)d_in[6], (const u16*)d_in[7], (const u16*)d_in[8], (u16*)d_out,
                     (unsigned char*)d_ws);
}
```

```cpp
#define TAILP 1
#include <hip/hip_runtime.h>
#include <stdint.h>

typedef unsigned short u16;
typedef unsigned v4u __attribute__((ext_vector_type(4)));
typedef unsigned v2u __attribute__((ext_vector_type(2)));
typedef float f32x4 __attribute__((ext_vector_type(4)));
typedef float f32x2 __attribute__((ext_vector_type(2)));
typedef short s16x4 __attribute__((ext_vector_type(4)));
typedef __bf16 bf16x8 __attribute__((ext_vector_type(8)));
typedef __bf16 bf16x2 __attribute__((ext_vector_type(2)));
#define LAS __attribute__((address_space(3)))
#define GAS __attribute__((address_space(1)))

constexpr int SK = 32768, WIN = 4096, DH = 128, DM = 512, FF = 1792;
constexpr int WS_FO = 4096;
constexpr int WS_FY = WS_FO + 32768;
constexpr int WS_X = WS_FY + 8192;
constexpr int WS_Y = WS_X + 262144;
constexpr int WS_Q0 = WS_Y + 524288;
constexpr int WS_GC = WS_Q0 + 131072;
constexpr int WS_GRP = WS_GC + 65536, GRP_SZ = 143360, O_L = 61440;
constexpr int GO_ML = 0;
constexpr int GO_O = 2048;
constexpr int GO_G = 51200;
constexpr int GO_ST = 59392;
constexpr int GO_ST2 = O_L + 61440 + 8192;
constexpr int GO_Q1 = O_L + 61440;
static_assert(GO_ST2 + 128 <= GRP_SZ && 128 * 480 == O_L, "group block");
constexpr int WS_BYTES = WS_GRP + 8 * GRP_SZ;
constexpr int OOB = 0x7fffff00;
constexpr int WS_STAMP = 8 << 20;
constexpr int L_IMG = 0;
constexpr int L_IGU = 16384;
constexpr int L_IDN = 32768;
constexpr int L_IWO = 40960;
constexpr int L_STG = 57344, STG_W = 8704;
constexpr int L_ATT = L_STG + 4 * STG_W;
constexpr int L_ML = L_ATT + 8192;
constexpr int L_QB = L_ML + 128, QSTR = 320;
constexpr int L_XB = L_QB + 4 * QSTR;
constexpr int XB_SZ = 384;
constexpr int L_HB = L_XB + 3 * XB_SZ;
constexpr int L_FL = L_HB + 1024;
constexpr int L_RED = L_FL + 256, RED_ROW = 144, RED_SZ = 4 * RED_ROW;
constexpr int LDS_BYTES = 112 * 1024;
static_assert(L_RED + 2 * RED_SZ <= LDS_BYTES, "lds");
constexpr int TW = 3;
constexpr unsigned SPIN_CAP = 1u << 17;

constexpr int DL[16] = {  105,   290,   420,   555,   675,   825,   970,   1090,
                          3,   1220,   1,   330,   8,
                          15,   4,   40};

constexpr int NOM_G = 85, NOM_Q = 92, NOM_O = 120;
#define SCHED() __builtin_amdgcn_sched_barrier(0)
#define STAMP(i) do {} while (0)
#define PASS(i) do {} while (0)
#define STAMP_FLUSH() do {} while (0)
#define DELAY(i) do { if (DL[i] > 0) __builtin_amdgcn_s_sleep(DL[i]); } while (0)
#define DLV(i) DL[i]
__device__ __forceinline__ unsigned rtc() { return (unsigned)__builtin_amdgcn_s_memrealtime(); }
#define WAIT_AT(ref, i) do { SCHED(); unsigned tg_ = (ref) + (unsigned)DLV(i); const unsigned lo_ = rtc() + (unsigned)DLV(13); if ((int)(lo_ - tg_) > 0) tg_ = lo_; \
    for (int k_ = 0; k_ < 4096 && (int)(rtc() - tg_) < 0; ++k_) {} SCHED(); } while (0)
#define PW_A 0
#define VPRE 0
#define PGT 0
#define DN_LATE 1
#define PW_B 1
#define UNLIKELY(x) __builtin_expect(!!(x), 0)
#define LIKELY(x) __builtin_expect(!!(x), 1)
#define PIN(v) asm volatile("" : "+v"(v))

__device__ __forceinline__ float bflo(unsigned u) { return __uint_as_float(u << 16); }
__device__ __forceinline__ float bfhi(unsigned u) { return __uint_as_float(u & 0xffff0000u); }
__device__ __forceinline__ unsigned pk(float lo, float hi) { bf16x2 p = {(__bf16)lo, (__bf16)hi}; return __builtin_bit_cast(unsigned, p); }
__device__ __forceinline__ float rbf(float f) { return bflo((unsigned)__builtin_bit_cast(u16, (__bf16)f)); }
__device__ __forceinline__ float vmax(float a, float b) { return __builtin_elementwise_maximum(a, b); }
template <int CTRL> __device__ __forceinline__ float dpp(float x) {
  return __builtin_bit_cast(float, __builtin_amdgcn_mov_dpp(__builtin_bit_cast(int, x), CTRL, 0xf, 0xf, true));
}
template <int CTRL> __device__ __forceinline__ unsigned dppu(unsigned x) { return (unsigned)__builtin_amdgcn_mov_dpp((int)x, CTRL, 0xf, 0xf, true); }
constexpr int XOR1 = 0xB1, XOR2 = 0x4E, HMIR = 0x141, MIR = 0x140, ROR4 = 0x124, ROR8 = 0x128;
__device__ __forceinline__ float row_sum(float v) { v += dpp<XOR1>(v); v += dpp<XOR2>(v); v += dpp<HMIR>(v); v += dpp<MIR>(v); return v; }
__device__ __forceinline__ float row_max(float v) { v = vmax(v, dpp<XOR1>(v)); v = vmax(v, dpp<XOR2>(v)); v = vmax(v, dpp<HMIR>(v)); v = vmax(v, dpp<MIR>(v)); return v; }
__device__ __forceinline__ float xrow_sum(float x) {
  auto s = __builtin_amdgcn_permlane16_swap(__float_as_uint(x), __float_as_uint(x), false, false);
  x = __uint_as_float(s[0]) + __uint_as_float(s[1]);
  auto t = __builtin_amdgcn_permlane32_swap(__float_as_uint(x), __float_as_uint(x), false, false);
  return __uint_as_float(t[0]) + __uint_as_float(t[1]);
}
__device__ __forceinline__ float xrow_max(float x) {
  auto s = __builtin_amdgcn_permlane16_swap(__float_as_uint(x), __float_as_uint(x), false, false);
  x = vmax(__uint_as_float(s[0]), __uint_as_float(s[1]));
  auto t = __builtin_amdgcn_permlane32_swap(__float_as_uint(x), __float_as_uint(x), false, false);
  return vmax(__uint_as_float(t[0]), __uint_as_float(t[1]));
}
__device__ __forceinline__ float rows_reduce4(f32x4 v) {
  auto a = __builtin_amdgcn_permlane16_swap(__float_as_uint(v[0]), __float_as_uint(v[1]), false, false);
  auto b = __builtin_amdgcn_permlane16_swap(__float_as_uint(v[2]), __float_as_uint(v[3]), false, false);
  const float t01 = __uint_as_float(a[0]) + __uint_as_float(a[1]), t23 = __uint_as_float(b[0]) + __uint_as_float(b[1]);
  auto c = __builtin_amdgcn_permlane32_swap(__float_as_uint(t01), __float_as_uint(t23), false, false);
  return __uint_as_float(c[0]) + __uint_as_float(c[1]);
}
__device__ __forceinline__ f32x4 mfma32(v4u a, v4u b, f32x4 c) {
  return __builtin_amdgcn_mfma_f32_16x16x32_bf16(__builtin_bit_cast(bf16x8, a), __builtin_bit_cast(bf16x8, b), c, 0, 0, 0);
}
__device__ __forceinline__ f32x4 mfma16(v2u a, v2u b, f32x4 c) {
  return __builtin_amdgcn_mfma_f32_16x16x16bf16_1k(__builtin_bit_cast(s16x4, a), __builtin_bit_cast(s16x4, b), c, 0, 0, 0);
}
__device__ __forceinline__ float dot2(unsigned a, unsigned b, float c) {
  return __builtin_amdgcn_fdot2_f32_bf16(__builtin_bit_cast(bf16x2, a), __builtin_bit_cast(bf16x2, b), c, false);
}
__device__ __forceinline__ v2u trread(LAS unsigned char* p) { return __builtin_bit_cast(v2u, __builtin_amdgcn_ds_read_tr16_b64_v4i16((LAS s16x4*)p)); }
__device__ __forceinline__ v4u ldg_nt(const u16* p) { return __builtin_nontemporal_load((const GAS v4u*)p); }
__device__ __forceinline__ v4u ld_sc1(__amdgpu_buffer_rsrc_t rs, int off) { return __builtin_amdgcn_raw_buffer_load_b128(rs, off, 0, 16); }
__device__ __forceinline__ unsigned ld1_sc1(__amdgpu_buffer_rsrc_t rs, int off) { return __builtin_amdgcn_raw_buffer_load_b32(rs, off, 0, 16); }
__device__ __forceinline__ v2u ld2_sc1(__amdgpu_buffer_rsrc_t rs, int off) { return __builtin_amdgcn_raw_buffer_load_b64(rs, off, 0, 16); }
__device__ __forceinline__ void st2_sc1(__amdgpu_buffer_rsrc_t rs, int off, v2u v) { __builtin_amdgcn_raw_buffer_store_b64(v, rs, off, 0, 16); }
__device__ __forceinline__ void st_sc1(__amdgpu_buffer_rsrc_t rs, int off, v4u v) { __builtin_amdgcn_raw_buffer_store_b128(v, rs, off, 0, 16); }
__device__ __forceinline__ void st1_sc1(__amdgpu_buffer_rsrc_t rs, int off, unsigned v) { __builtin_amdgcn_raw_buffer_store_b32(v, rs, off, 0, 16); }
#define PERM(hi, lo, sel) __builtin_amdgcn_perm((hi), (lo), (sel))
__device__ __forceinline__ bool okH(v4u d, unsigned T) { return (PERM(d.y, d.x, 0x05040100u) == T) & (PERM(d.w, d.z, 0x05040100u) == T); }
__device__ __forceinline__ v2u valH(v4u d) { return v2u{PERM(d.y, d.x, 0x07060302u), PERM(d.w, d.z, 0x07060302u)}; }
__device__ __forceinline__ v4u mkH(unsigned p0, unsigned p1, unsigned T) {
  return v4u{PERM(p0, T, 0x05040100u), PERM(p0, T, 0x07060302u), PERM(p1, T, 0x05040100u), PERM(p1, T, 0x07060302u)};
}
__device__ __forceinline__ v4u unF(v4u d, unsigned T) { return v4u{d.x ^ (T & 0xffu), d.y ^ ((T >> 8) & 0xffu), d.z ^ ((T >> 16) & 0xffu), d.w ^ (T >> 24)}; }
__device__ __forceinline__ unsigned or4(v4u d) { return (d.x | d.y) | (d.z | d.w); }
__device__ __forceinline__ v4u mkF(float a, float b, float c, float d, unsigned T) {
  return v4u{PERM(__float_as_uint(a), T, 0x07060500u), PERM(__float_as_uint(b), T, 0x07060501u),
             PERM(__float_as_uint(c), T, 0x07060502u), PERM(__float_as_uint(d), T, 0x07060503u)};
}
#define LGKM0() asm volatile("s_waitcnt lgkmcnt(0)" ::: "memory")
#define BARRIER() do { LGKM0(); __builtin_amdgcn_s_barrier(); asm volatile("" ::: "memory"); } while (0)

__device__ __forceinline__ float gemv128(v2u a0, v2u a1, const v2u (&B)[8]) {
  const f32x4 z = {0.f, 0.f, 0.f, 0.f};
  f32x4 c0 = mfma16(a0, B[0], z), c1 = mfma16(a0, B[1], z), c2 = mfma16(a0, B[2], z), c3 = mfma16(a0, B[3], z);
  c0 = mfma16(a1, B[4], c0); c1 = mfma16(a1, B[5], c1); c2 = mfma16(a1, B[6], c2); c3 = mfma16(a1, B[7], c3);
  return (c0[0] + c1[1]) + (c2[2] + c3[3]);
}
struct Ctx {
  const u16 *vc, *wo, *wg, *wu, *wd; u16* out; unsigned char* ws;
  __amdgpu_buffer_rsrc_t rs; LAS unsigned char* L;
  int lane, w, wv, kg, i16, b, g, c; unsigned E, T, t0, tref;
};

#define LPOST(set) do { asm volatile("" ::: "memory"); *(volatile LAS unsigned*)(L + L_FL + (set) * 16 + w * 4) = T; asm volatile("" ::: "memory"); } while (0)
#define LWAIT4(set) do { for (unsigned spins_ = 0; spins_ < (1u << 24); ++spins_) { const v4u f_ = *(volatile LAS v4u*)(L + L_FL + (set) * 16); \
      if ((f_.x == T) & (f_.y == T) & (f_.z == T) & (f_.w == T)) break; } asm volatile("" ::: "memory"); } while (0)
#define LWAIT1(set, who) do { for (unsigned spins_ = 0; spins_ < (1u << 24); ++spins_) { \
      if (*(volatile LAS unsigned*)(L + L_FL + (set) * 16 + (who) * 4) == T) break; __builtin_amdgcn_s_sleep(1); } asm volatile("" ::: "memory"); } while (0)
#define ONES 0x3f803f80u
#define FL4(set) (*(volatile LAS v4u*)(L + L_FL + (set) * 16))
#define FL4OK(f) (((f).x == T) & ((f).y == T) & ((f).z == T) & ((f).w == T))

__device__ __forceinline__ v4u load_sw(const Ctx& X, int n) {
  const int cb = (X.c & 7) < 7 ? (X.c & 7) : 6;
  if (n < 4) return ldg_nt(X.wo + (size_t)(512 * X.g + 256 * (X.c & 1) + 64 * X.w + 16 * n + (X.lane >> 2)) * DM + 32 * (X.c >> 1) + 8 * (X.lane & 3));
  if (n < 8) return ldg_nt((n >= 6 ? X.wu : X.wg) + (size_t)(128 * (X.c >> 3) + 32 * X.w + 16 * (n & 1) + (X.lane >> 2)) * FF + 224 * X.g + 32 * cb + 8 * (X.lane & 3));
  const int blk = 2 * X.w + (n & 1) < 7 ? 2 * X.w + (n & 1) : 6;
  return ldg_nt(X.wd + (size_t)(224 * X.g + 32 * blk + (X.lane >> 1)) * DM + 16 * X.c + 8 * (X.lane & 1));
}
__device__ __forceinline__ void run_A(Ctx& X, v4u (&Kf)[8], v4u (&Vf)[8], v4u (&Sw)[10]) {
  const int lane = X.lane, w = X.w, kg = X.kg, i16 = X.i16, g = X.g, c = X.c;
  const __amdgpu_buffer_rsrc_t rs = X.rs; LAS unsigned char* L = X.L; LAS unsigned char* stg = L + L_STG + w * STG_W;
  const f32x4 z = {0.f, 0.f, 0.f, 0.f};
  STAMP(12);
  const int u = (lane * 43) >> 7;
  const bool head3 = lane == 3 * u;
  int ooff = head3 ? WS_GRP + g * GRP_SZ + GO_O + w * 12288 + u * 512 + c * 16 : (lane == 1 ? WS_GRP + g * GRP_SZ + GO_ML + w * 512 + c * 16 : OOB);
  const int ostep = (head3 || lane == 1) ? O_L : 0;
  const u16* vb = X.vc + ((size_t)g * SK + (SK - WIN) + 128 * c + 32 * w) * DH;
  {
#pragma unroll
    for (int s = 0; s < 8; ++s) {
      *(LAS v4u*)(stg + (4 * s + kg) * 272 + i16 * 16) = Kf[s];
      SCHED();
      if (s + VPRE < 8) { const int j = s + VPRE; Vf[j] = ldg_nt(vb + (16 * (j >> 2) + 4 * kg + (j & 3)) * DH + 8 * i16); }
      SCHED();
    }
    LGKM0();
#pragma unroll
    for (int s = 0; s < 8; ++s) Kf[s] = *(LAS v4u*)(stg + (16 * (s >> 2) + i16) * 272 + (s & 3) * 64 + kg * 16);
  }
  {
    v4u t[8];
#pragma unroll
    for (int jj = 0; jj < 4; ++jj) {
#pragma unroll
      for (int n = 0; n < 8; ++n) t[n][jj] = PERM(Vf[2 * jj + 1][n >> 1], Vf[2 * jj][n >> 1], (n & 1) ? 0x07060302u : 0x05040100u);
      asm volatile("" : "+v"(t[0][jj]), "+v"(t[7][jj]));
      SCHED();
      Sw[jj] = load_sw(X, jj);
      if (TAILP >= 1) Sw[4 + jj] = load_sw(X, 4 + jj);
      if (TAILP >= 2 && jj >= 2) Sw[6 + jj] = load_sw(X, 6 + jj);
      SCHED();
    }
#pragma unroll
    for (int n = 0; n < 8; ++n) Vf[n] = t[n];
    asm volatile("" :: "v"(Vf[0]), "v"(Vf[1]), "v"(Vf[2]), "v"(Vf[3]), "v"(Vf[4]), "v"(Vf[5]), "v"(Vf[6]), "v"(Vf[7]));
  }
  STAMP(13);
#pragma unroll
  for (int n = 0; n < 4; ++n) { *(LAS v4u*)(L + L_IWO + w * 4096 + n * 1024 + lane * 16) = Sw[n]; SCHED(); if (TAILP < 1) Sw[4 + n] = load_sw(X, 4 + n); if (TAILP == 1 && n < 2) Sw[8 + n] = load_sw(X, 8 + n); SCHED(); }
#pragma unroll
  for (int n = 0; n < 4; ++n) {
    *(LAS v4u*)(L + L_IGU + (n >> 1) * 8192 + w * 2048 + (n & 1) * 1024 + lane * 16) = Sw[4 + n]; SCHED();
    if (TAILP < 1 && (n & 1)) { Sw[8 + (n >> 1)] = load_sw(X, 8 + (n >> 1)); SCHED(); }
  }
  if (DN_LATE == 0) {
#pragma unroll
    for (int n = 0; n < 2; ++n) *(LAS v4u*)(L + L_IDN + (2 * w + n) * 1024 + lane * 16) = Sw[8 + n];
  }
  STAMP(9);
#pragma unroll
  for (int layer = 0; layer < 2; ++layer) {
    BARRIER();
    const unsigned T = __builtin_amdgcn_readfirstlane(*(volatile LAS unsigned*)(L + L_FL));
    {
      v4u qf[4];
#pragma unroll
      for (int s = 0; s < 4; ++s) qf[s] = *(LAS v4u*)(L + L_QB + (i16 >> 2) * QSTR + s * 64 + kg * 16);
      f32x4 sa = z, sb = z;
#pragma unroll
      for (int s = 0; s < 4; ++s) { sa = mfma32(Kf[s], qf[s], sa); sb = mfma32(Kf[4 + s], qf[s], sb); }
      float sv[8];
      if (layer == 0) {
#pragma unroll
        for (int k = 0; k < 8; k += 2) {
          const unsigned p = k < 4 ? pk(sa[k & 3], sa[(k & 3) + 1]) : pk(sb[k & 3], sb[(k & 3) + 1]);
          const unsigned r = pk(bflo(p) * 0.08838834764831845f, bfhi(p) * 0.08838834764831845f);
          sv[k] = bflo(r); sv[k + 1] = bfhi(r);
        }
      } else {
#pragma unroll
        for (int k = 0; k < 4; ++k) { sv[k] = sa[k]; sv[4 + k] = sb[k]; }
      }
      const float CE = layer == 0 ? 1.4426950408889634f : 1.4426950408889634f * 0.08838834764831845f;
      const float m = xrow_max(vmax(vmax(vmax(sv[0], sv[1]), vmax(sv[2], sv[3])), vmax(vmax(sv[4], sv[5]), vmax(sv[6], sv[7])))) * CE;
#pragma unroll
      for (int k = 0; k < 8; ++k) sv[k] = __builtin_amdgcn_exp2f(fmaf(sv[k], CE, -m));
      const v4u pf = {pk(sv[0], sv[1]), pk(sv[2], sv[3]), pk(sv[4], sv[5]), pk(sv[6], sv[7])};
      f32x4 o[8];
#pragma unroll
      for (int n = 0; n < 8; ++n) o[n] = mfma32(pf, Vf[n], z);
      const f32x4 lv = mfma32(pf, v4u{ONES, ONES, ONES, ONES}, z);
      LAS unsigned char* op = L + L_ATT + w * 2048 + kg * 512 + i16 * 32;
      *(LAS f32x4*)op = f32x4{o[0][0], o[1][1], o[2][2], o[3][3]};
      *(LAS f32x4*)(op + 16) = f32x4{o[4][0], o[5][1], o[6][2], o[7][3]};
      if (i16 == 4 * kg) *(LAS f32x2*)(L + L_ML + kg * 32 + w * 8) = f32x2{m, lv[0]};
    }
    LPOST(1 + layer);
    const unsigned tm = rtc();
    {
      f32x4 ml0, ml1; f32x2 ov[4];
      for (unsigned sp = 0; sp < (1u << 24); ++sp) {
        const v4u f = FL4(1 + layer);
        ml0 = *(volatile LAS f32x4*)(L + L_ML + w * 32); ml1 = *(volatile LAS f32x4*)(L + L_ML + w * 32 + 16);
#pragma unroll
        for (int k = 0; k < 4; ++k) ov[k] = *(volatile LAS f32x2*)(L + L_ATT + k * 2048 + w * 512 + lane * 8);
        if (FL4OK(f)) break;
      }
      const float M = vmax(vmax(ml0[0], ml0[2]), vmax(ml1[0], ml1[2]));
      const float e0 = __builtin_amdgcn_exp2f(ml0[0] - M), e1 = __builtin_amdgcn_exp2f(ml0[2] - M), e2 = __builtin_amdgcn_exp2f(ml1[0] - M), e3 = __builtin_amdgcn_exp2f(ml1[2] - M);
      const f32x2 a = (ov[0] * e0 + ov[1] * e1) + (ov[2] * e2 + ov[3] * e3);
      const float ls = fmaf(e0, ml0[1], e1 * ml0[3]) + fmaf(e2, ml1[1], e3 * ml1[3]);
      unsigned dv = pk(a[0], a[1]);
      unsigned d1 = dppu<0x130>(dv);
      unsigned d2 = dppu<0x130>(d1);
      dv = head3 ? dv : __float_as_uint(M); d1 = head3 ? d1 : __float_as_uint(ls); d2 = head3 ? d2 : tm;
      st_sc1(rs, ooff, v4u{dv, d1, d2, T});
      ooff += ostep; asm volatile("" : "+v"(ooff));
    }
    if (layer == 0) { STAMP(3);
      if (DN_LATE) {
#pragma unroll
        for (int n = 0; n < 2; ++n) *(LAS v4u*)(L + L_IDN + (2 * w + n) * 1024 + lane * 16) = Sw[8 + n];
      }
      LPOST(15);
    } else STAMP(15);
  }
}

#define RETRY(pi, reload) if (LIKELY(__all(ok)) || spins > SPIN_CAP) break; asm volatile("" ::: "memory"); DELAY(8); PASS(pi); if (!ok) { reload; }
#define SLIP(i, lat) do { const int late_ = (int)(rtc() - (X.tref + (unsigned)DLV(i) + (unsigned)(lat))); if (late_ > 0) X.tref += (unsigned)(late_ + DLV(15)); } while (0)
#define RESYNC(tmax, i, nom) do { const int late_ = (int)((tmax) - (X.tref + (unsigned)DLV(i) + (unsigned)(nom))); if (late_ > 0) X.tref += (unsigned)late_; } while (0)
#define QUAD_TMAX(tv) ({ int dt_ = (int)((tv) - X.tref); dt_ = max(dt_, (int)dppu<XOR1>((unsigned)dt_)); dt_ = max(dt_, (int)dppu<XOR2>((unsigned)dt_)); X.tref + (unsigned)__builtin_amdgcn_readfirstlane(dt_); })
#define LN_STATS(ds) float s1 = __uint_as_float((ds).x), s2 = __uint_as_float((ds).y); \
      s1 += dpp<XOR1>(s1); s2 += dpp<XOR1>(s2); s1 += dpp<XOR2>(s1); s2 += dpp<XOR2>(s2); \
      const float mean = s1 * (1.f / DM), rstd = __builtin_amdgcn_rsqf(fmaxf(fmaf(-mean, mean, s2 * (1.f / DM)), 0.f) + 1e-5f)

template <int layer>
__device__ __forceinline__ void run_B(Ctx& X, const v2u (&Bq)[8], v4u (&Aw)[12], v2u (&Bg)[8], v4u (&Bd)[7], float& xr, f32x4& csq, float (&csg)[2]) {
  const int lane = X.lane, w = X.w, kg = X.kg, i16 = X.i16, g = X.g, c = X.c, kq = c >> 3;
  const unsigned T = X.T; const __amdgpu_buffer_rsrc_t rs = X.rs; LAS unsigned char* L = X.L;
  LAS unsigned char* red = L + L_RED + layer * RED_SZ;
  const f32x4 z = {0.f, 0.f, 0.f, 0.f};
  const int grp = WS_GRP + g * GRP_SZ;
  v2u fy0; v4u c0, c1; unsigned tq;
  const int qun = kg * 8 + (lane & 7), qidx = ((lane >> 3) & 1) * 2048 + (32 * w + qun) * 16;
  const int xun = 8 * w + (lane & 7);
  {
    int qoff = (layer == 0 ? WS_Q0 + g * 8192 : grp + GO_Q1) + qidx, soff = grp + GO_ST2 + (lane & 3) * 16;
    PIN(qoff); PIN(soff);
    if (layer == 0) WAIT_AT(X.t0, 11); else WAIT_AT(X.tref, 4);
    v4u ds = v4u{0u, 0u, 0u, T}, r0, r1;
#define QLOADS() do { if (layer == 1) ds = ld_sc1(rs, soff); r0 = ld_sc1(rs, qoff); r1 = ld_sc1(rs, qoff + 4096); } while (0)
    QLOADS();
    SCHED();
    for (unsigned spins = 0;; ++spins) {
      LN_STATS(ds);
      const v4u d0 = unF(r0, T), d1 = unF(r1, T);
      float qv[4];
#pragma unroll
      for (int k = 0; k < 4; ++k) {
        float t = __uint_as_float(d0[k]) + __uint_as_float(d1[k]); t += dpp<ROR8>(t);
        qv[k] = layer == 0 ? t : rstd * fmaf(-mean, csq[k], t);
      }
      *(LAS v2u*)(L + L_QB + w * QSTR + qun * 8) = v2u{pk(qv[0], qv[1]), pk(qv[2], qv[3])};
      const bool ok = (((or4(d0) | or4(d1)) & 0xffu) == 0u) & (ds.w == T);
      RETRY(layer ? 7 : 0, QLOADS());
    }
    tq = ds.z;
    STAMP(2 + 12 * layer);
  }
  BARRIER();
  if (layer == 1) { RESYNC(QUAD_TMAX(tq), 3, NOM_Q); SLIP(4, 72); }
  if (layer == 0) {
    tq = rtc();
    if (w == TW && lane == 0) st2_sc1(rs, WS_FO + (g * 32 + c) * 128, v2u{T, tq});
    fy0 = ld2_sc1(rs, w == TW ? WS_FO + (g * 32 + (lane & 31)) * 128 : OOB);
    c0 = ld_sc1(rs, WS_Q0 + 65536 + g * 8192 + qidx); c1 = ld_sc1(rs, WS_Q0 + 65536 + g * 8192 + qidx + 4096);
    SCHED();
  }
#define WO_PREP() do { const int hh = w >> 1; \
_Pragma("unroll") \
    for (int t = 0; t < 6; ++t) { \
      const int un = 4 * t + kg, r0 = 6 * un + (i16 >> 2); \
      const int ra = r0 > 127 ? 127 : r0, rb = r0 + 4 > 127 ? 127 : r0 + 4; \
_Pragma("unroll") \
      for (int ct = 0; ct < 2; ++ct) { \
        const v2u lo = trread(L + L_IWO + (hh * 128 + ra) * 64 + ct * 32 + (i16 & 3) * 8), hi = trread(L + L_IWO + (hh * 128 + rb) * 64 + ct * 32 + (i16 & 3) * 8); \
        Aw[t * 2 + ct] = v4u{(t == 5 && un > 21) ? 0u : lo.x, (t == 5 && un > 20) ? 0u : lo.y, (t == 5 && un > 20) ? 0u : hi.x, 0u}; \
      } \
    } \
  } while (0)
  if (layer == 0) {
    WO_PREP();
#pragma unroll
    for (int u = 0; u < 8; ++u) Bg[u] = trread(L + L_IGU + (w >> 1) * 8192 + (64 * (u >> 2) + 16 * kg + 4 * (u & 3) + (i16 >> 2)) * 64 + (w & 1) * 32 + (i16 & 3) * 8);
    const v2u one = {ONES, ONES};
    const float cs = gemv128(one, one, Bg);
    const float p1 = dpp<0x55>(cs), p2 = dpp<0xAA>(cs), p3 = dpp<0xFF>(cs);
    st_sc1(rs, ((lane & 0x33) == 0 && (c & 7) < 7) ? WS_GC + g * 8192 + kq * 2048 + (w >> 1) * 1024 + (8 * (c & 7) + 4 * (w & 1) + (i16 >> 2)) * 16 : OOB, mkF(cs, p1, p2, p3, T));

  }
  {
    const int hh = w >> 1, st = w & 1, h = 2 * (c & 1) + hh;
    int obase = grp + GO_O + layer * O_L + h * 12288 + (16 * st + i16) * 16 + kg * 512;
    int o5 = obase + 5 * 2048 - (kg & 2) * 512;
    const int rot = (4 * kg + i16) & 15;
    int mlA = grp + GO_ML + layer * O_L + h * 512 + (16 * st + rot) * 16, mlB = mlA ^ 256;
    const int slot = ((lane >> 4) & 1) * 4 + (lane & 3), rep = ((lane >> 2) & 3) + 4 * (lane >> 5);
    int yst = WS_Y + layer * 262144 + rep * 32768 + (2 * g + (c & 1)) * 2048 + (c >> 1) * 128 + slot * 16;
    PIN(obase); PIN(o5); PIN(mlA); PIN(mlB); PIN(yst);
    if (layer == 0) {
      unsigned tg;
      if (w == TW) {
        for (unsigned spins = 0;; ++spins) {
          if ((lane & 31) == c) fy0 = v2u{T, tq};
          if (__all(fy0.x == T) || spins > SPIN_CAP) break;
          asm volatile("" ::: "memory"); DELAY(10); PASS(1);
          fy0 = ld2_sc1(rs, WS_FO + (g * 32 + (lane & 31)) * 128);
        }
        int dt = (int)(fy0.y - X.t0); dt = max(dt, (int)dppu<XOR1>((unsigned)dt)); dt = max(dt, (int)dppu<XOR2>((unsigned)dt)); dt = max(dt, (int)dppu<HMIR>((unsigned)dt));
        dt = max(dt, (int)dppu<MIR>((unsigned)dt)); dt = max(__builtin_amdgcn_readlane(dt, 0), __builtin_amdgcn_readlane(dt, 16)); tg = X.t0 + (unsigned)dt;
        *(volatile LAS unsigned*)(L + L_FL + 3 * 16) = tg; LPOST(3);
      } else { LWAIT1(3, TW); tg = __builtin_amdgcn_readfirstlane(*(volatile LAS unsigned*)(L + L_FL + 3 * 16)); }
      if (c == 0 && w == TW && lane < 8) st2_sc1(rs, WS_FY + (lane * 8 + g) * 128, v2u{T, tg});
      for (unsigned spins = 0;; ++spins) {
        c0 = unF(c0, T); c1 = unF(c1, T);
        if (__all(((or4(c0) | or4(c1)) & 0xffu) == 0u) || spins > SPIN_CAP) break;
        asm volatile("" ::: "memory"); DELAY(8); c0 = ld_sc1(rs, WS_Q0 + 65536 + g * 8192 + qidx); c1 = ld_sc1(rs, WS_Q0 + 65536 + g * 8192 + qidx + 4096);
      }
#pragma unroll
      for (int k = 0; k < 4; ++k) { float t = __uint_as_float(c0[k]) + __uint_as_float(c1[k]); t += dpp<ROR8>(t); csq[k] = t; }
      { unsigned t_ = tg + (unsigned)DLV(0); for (int k_ = 0; k_ < 4096 && (int)(rtc() - t_) < 0; ++k_) {} }
      X.tref = tg;
    } else WAIT_AT(X.tref, 5);
    if (layer == 0) fy0 = ld2_sc1(rs, WS_FY + (g * 8 + (lane & 7)) * 128);
    v4u d[8];
#define OLOADS() do { d[6] = ld_sc1(rs, mlA); d[7] = ld_sc1(rs, mlB); _Pragma("unroll") for (int t = 0; t < 5; ++t) d[t] = ld_sc1(rs, obase + t * 2048); d[5] = ld_sc1(rs, o5); } while (0)
    OLOADS();
    SCHED();
    for (unsigned spins = 0;; ++spins) {
      const float mA = __uint_as_float(d[6].x), lA = __uint_as_float(d[6].y), mB = __uint_as_float(d[7].x), lB = __uint_as_float(d[7].y);
      const float M = row_max(vmax(mA, mB));
      const float eA = __builtin_amdgcn_exp2f(mA - M), eB = __builtin_amdgcn_exp2f(mB - M);
      const float Ls = row_sum(fmaf(eA, lA, eB * lB));
      const float wgt = eA * __builtin_amdgcn_rcpf(Ls);
      const float w0 = dpp<0x150>(wgt), w1 = dpp<0x151>(wgt), w2 = dpp<0x152>(wgt), w3 = dpp<0x153>(wgt);
      f32x4 a0 = z, a1 = z;
#pragma unroll
      for (int t = 0; t < 6; ++t) { a0 = mfma32(d[t], Aw[2 * t], a0); a1 = mfma32(d[t], Aw[2 * t + 1], a1); }
      const float y0 = fmaf(a0[0], w0, a0[1] * w1) + fmaf(a0[2], w2, a0[3] * w3), y1 = fmaf(a1[0], w0, a1[1] * w1) + fmaf(a1[2], w2, a1[3] * w3);
      auto sw = __builtin_amdgcn_permlane16_swap(__float_as_uint(y0), __float_as_uint(y1), false, false);
      const float yy = __uint_as_float(sw[0]) + __uint_as_float(sw[1]);
      auto sx = __builtin_amdgcn_permlane32_swap(__float_as_uint(yy), __float_as_uint(yy), false, false);
      *(LAS float*)(red + w * RED_ROW + (lane & 31) * 4) = __uint_as_float(sx[0]) + __uint_as_float(sx[1]);
      const bool ok = (d[0].w == T) & (d[1].w == T) & (d[2].w == T) & (d[3].w == T) & (d[4].w == T) & (d[5].w == T) & (d[6].w == T) & (d[7].w == T);
      RETRY(layer ? 8 : 1, OLOADS());
    }
    if (layer == 0) asm volatile("s_waitcnt vmcnt(0)" ::: "memory");
    LPOST(4 + 5 * layer);
    STAMP(4 + 12 * layer);
    if (w == TW) {
      f32x4 v;
      for (unsigned sp = 0; sp < (1u << 24); ++sp) {
        const v4u f = FL4(4 + 5 * layer);
        v = *(volatile LAS f32x4*)(red + ((lane >> 2) & 3) * RED_ROW + slot * 16);
        if (FL4OK(f)) break;
      }
#pragma unroll
      for (int k = 0; k < 4; ++k) { float t = v[k]; t += dpp<ROR8>(t); t += dpp<ROR4>(t); v[k] = t; }
      st_sc1(rs, yst, mkF(v[0], v[1], v[2], v[3], T));
    }
    if (layer == 1) {
      int dt = max((int)(d[6].z - X.tref), (int)(d[7].z - X.tref));
      dt = max(dt, (int)dppu<XOR1>((unsigned)dt)); dt = max(dt, (int)dppu<XOR2>((unsigned)dt)); dt = max(dt, (int)dppu<HMIR>((unsigned)dt)); dt = max(dt, (int)dppu<MIR>((unsigned)dt));
      RESYNC(X.tref + (unsigned)__builtin_amdgcn_readfirstlane(dt), 4, NOM_O);
      SLIP(5, 125);
    }
  }
  STAMP(5 + 12 * layer);
#define X_SHARE(xb, setx, sets) do { LPOST(setx); \
    float s1 = xr, s2 = xr * xr; \
    s1 += dpp<XOR1>(s1); s2 += dpp<XOR1>(s2); s1 += dpp<XOR2>(s1); s2 += dpp<XOR2>(s2); s1 += dpp<HMIR>(s1); s2 += dpp<HMIR>(s2); \
    *(LAS f32x2*)((xb) + 256 + (4 * w + kg) * 8) = f32x2{s1, s2}; LPOST(sets); } while (0)
#define X_FETCH(xb, setx) v2u a0, a1; for (unsigned sp = 0; sp < (1u << 24); ++sp) { const v4u f = FL4(setx); \
      a0 = *(volatile LAS v2u*)((xb) + (4 * kg + (i16 & 3)) * 8); a1 = *(volatile LAS v2u*)((xb) + 128 + (4 * kg + (i16 & 3)) * 8); if (FL4OK(f)) break; }
#define STATS_PUBLISH(xb, sets, idx) do { f32x2 sp2; for (unsigned sp = 0; sp < (1u << 24); ++sp) { const v4u f = FL4(sets); sp2 = *(volatile LAS f32x2*)((xb) + 256 + i16 * 8); if (FL4OK(f)) break; } \
    const float S1 = row_sum(sp2[0]), S2 = row_sum(sp2[1]); \
    st_sc1(rs, lane == 0 ? grp + (idx) + kq * 16 : OOB, v4u{__float_as_uint(S1), __float_as_uint(S2), rtc(), T}); } while (0)
  {
    const int go = kq * 2048 + (w >> 1) * 1024 + (8 * (c & 7) + 4 * (w & 1) + (i16 >> 2)) * 16;
    const bool gst = (lane & 0x33) == 0 && (c & 7) < 7;
    int goff = gst ? grp + GO_G + layer * O_L + go : OOB;
    int yoff = WS_Y + layer * 262144 + g * 32768 + (lane >> 3) * 2048 + (32 * kq + xun) * 16;
    PIN(goff); PIN(yoff);
    if (layer == 0) {
      {
        for (unsigned spins = 0;; ++spins) {
          if ((lane & 7) == g) fy0 = v2u{T, X.tref};
          if (__all(fy0.x == T) || spins > SPIN_CAP) break;
          asm volatile("" ::: "memory"); DELAY(14); PASS(3);
          fy0 = ld2_sc1(rs, WS_FY + (g * 8 + (lane & 7)) * 128);
        }
        int dt = (int)(fy0.y - X.t0); dt = max(dt, (int)dppu<XOR1>((unsigned)dt)); dt = max(dt, (int)dppu<XOR2>((unsigned)dt)); dt = max(dt, (int)dppu<HMIR>((unsigned)dt));
        X.tref = X.t0 + (unsigned)__builtin_amdgcn_readfirstlane(dt);
      }
      { unsigned t_ = X.tref + (unsigned)DLV(1); for (int k_ = 0; k_ < 4096 && (int)(rtc() - t_) < 0; ++k_) {} }
    } else WAIT_AT(X.tref, 6);
    LAS unsigned char* xb = L + L_XB + 2 * layer * XB_SZ;
    float xn;
    v4u r0 = ld_sc1(rs, yoff), r1 = ld_sc1(rs, yoff + 16384);
    SCHED();
    for (unsigned spins = 0;; ++spins) {
      const v4u d0 = unF(r0, T), d1 = unF(r1, T);
      float t = rows_reduce4(__builtin_bit_cast(f32x4, d0) + __builtin_bit_cast(f32x4, d1));
      t += dpp<ROR8>(t);
      const unsigned p = pk(xr + t, 0.f);
      *(LAS u16*)(xb + (4 * xun + kg) * 2) = (u16)p;
      xn = bflo(p);
      const bool ok = ((or4(d0) | or4(d1)) & 0xffu) == 0u;
      RETRY(layer ? 9 : 4, r0 = ld_sc1(rs, yoff); r1 = ld_sc1(rs, yoff + 16384));
    }
    xr = xn;
    STAMP(6 + 12 * layer);
    X_SHARE(xb, 6 + 4 * layer, 12 + layer);
    if ((c & 7) < 7) {
      X_FETCH(xb, 6 + 4 * layer);
      const float part = gemv128(a0, a1, Bg);
      const float p1 = dpp<0x55>(part), p2 = dpp<0xAA>(part), p3 = dpp<0xFF>(part);
      st_sc1(rs, goff, mkF(part, p1, p2, p3, T));
    } else if (w == 0) STATS_PUBLISH(xb, 12 + layer, GO_ST + layer * O_L);
  }
  if (layer == 1) STAMP(19);
  SLIP(layer == 0 ? 1 : 6, 98);
  if (layer == 0) {
    const v4u zz = {0u, 0u, 0u, 0u};
    const int slot = ((lane >> 4) & 1) * 4 + (lane & 3), rep = ((lane >> 2) & 3) + 4 * (lane >> 5);
    if (w == TW) {
      st_sc1(rs, WS_Y + 262144 + rep * 32768 + (2 * g + (c & 1)) * 2048 + (c >> 1) * 128 + slot * 16, zz);
      st_sc1(rs, i16 < 8 ? WS_X + 131072 + i16 * 16384 + g * 2048 + c * 64 + kg * 16 : OOB, zz);
    }
    st_sc1(rs, lane < 30 ? grp + O_L + (c * 4 + w) * 480 + lane * 16
                         : ((lane & 0x33) == 0x20 ? grp + GO_Q1 + kq * 2048 + ((c & 7) * 64 + w * 16 + i16) * 4 : ((lane == 63 && (c & 7) == 0 && w == 0) ? grp + GO_ST2 + kq * 16 : OOB)), zz);
  }
  {
    const int hu = 14 * w + (i16 < 14 ? i16 : 13);
    const int gi = kg * 2048 + hu * 16;
    int gld = grp + GO_G + layer * O_L + gi, soff = grp + GO_ST + layer * O_L + (lane & 3) * 16;
    int xst = i16 < 8 ? WS_X + layer * 131072 + i16 * 16384 + g * 2048 + c * 64 + kg * 16 : OOB;
    PIN(gld); PIN(soff); PIN(xst);
    if (layer == 0 && w == TW) {
      LWAIT4(15);
#pragma unroll
      for (int t = 0; t < 7; ++t) {
        const int r0 = 32 * t + 8 * kg + (i16 >> 2);
        const v2u lo = trread(L + L_IDN + r0 * 32 + (i16 & 3) * 8), hi = trread(L + L_IDN + (r0 + 4) * 32 + (i16 & 3) * 8);
        Bd[t] = v4u{lo.x, lo.y, hi.x, hi.y};
      }
    }
    WAIT_AT(X.tref, layer == 0 ? 2 : 7);
    LAS unsigned char* hb = L + L_HB + layer * 512;
    v4u ds, r0, r1;
#define GLOADS() do { ds = ld_sc1(rs, soff); if (layer == 0) { c0 = ld_sc1(rs, WS_GC + g * 8192 + gi); c1 = ld_sc1(rs, WS_GC + g * 8192 + gi + 1024); } \
      r0 = ld_sc1(rs, gld); r1 = ld_sc1(rs, gld + 1024); } while (0)
    GLOADS();
    SCHED();
    for (unsigned spins = 0;; ++spins) {
      LN_STATS(ds);
      unsigned cbad = 0u;
      if (layer == 0) {
        const v4u e0 = unF(c0, T), e1 = unF(c1, T); cbad = or4(e0) | or4(e1);
        csg[0] = rows_reduce4(__builtin_bit_cast(f32x4, e0)); csg[1] = rows_reduce4(__builtin_bit_cast(f32x4, e1));
      }
      const v4u d0 = unF(r0, T), d1 = unF(r1, T);
      const float gv = rstd * fmaf(-mean, csg[0], rows_reduce4(__builtin_bit_cast(f32x4, d0)));
      const float uv = rstd * fmaf(-mean, csg[1], rows_reduce4(__builtin_bit_cast(f32x4, d1)));
      const float hv = uv * __builtin_amdgcn_rcpf(1.f + __builtin_amdgcn_exp2f(-1.4426950408889634f * gv));
      *(LAS u16*)(hb + (4 * hu + kg) * 2) = (u16)pk(hv, 0.f);
      const bool ok = (((or4(d0) | or4(d1) | cbad) & 0xffu) == 0u) & (ds.w == T);
      RETRY(5, GLOADS());
    }
    if (layer == 0) asm volatile("s_waitcnt vmcnt(0)" ::: "memory");
    LPOST(7 + 4 * layer);
    STAMP(8 + 12 * layer);
    if (w == TW) {
      v4u hf[7];
      for (unsigned sp = 0; sp < (1u << 24); ++sp) {
        const v4u f = FL4(7 + 4 * layer);
#pragma unroll
        for (int t = 0; t < 7; ++t) hf[t] = *(volatile LAS v4u*)(hb + t * 64 + kg * 16);
        if (FL4OK(f)) break;
      }
      f32x4 a0 = z, a1 = z;
#pragma unroll
      for (int t = 0; t < 7; ++t) { if (t & 1) a1 = mfma32(Bd[t], hf[t], a1); else a0 = mfma32(Bd[t], hf[t], a0); }
      a0 += a1;
      st_sc1(rs, xst, mkF(a0[0], a0[1], a0[2], a0[3], T));
    }
    if (layer == 0) STAMP(10);
    RESYNC(QUAD_TMAX(ds.z), layer == 0 ? 1 : 6, NOM_G);
    SLIP(layer == 0 ? 2 : 7, layer == 0 ? 125 : 105);
  }
  if (layer == 0 || (g == 0 && (c & 7) == 0)) {
    int xoff = WS_X + layer * 131072 + g * 16384 + (lane >> 3) * 2048 + (32 * kq + xun) * 16;
    int qst = (lane & 0x33) == 0 ? grp + GO_Q1 + kq * 2048 + ((c & 7) * 64 + w * 16 + i16) * 4 : OOB;
    PIN(xoff); PIN(qst);
    WAIT_AT(X.tref, layer == 0 ? 3 : 9);
    LAS unsigned char* xb = L + L_XB + XB_SZ;
    unsigned p;
    v4u r0 = ld_sc1(rs, xoff);
    for (unsigned spins = 0;; ++spins) {
      const v4u d0 = unF(r0, T);
      float t = rows_reduce4(__builtin_bit_cast(f32x4, d0));
      t += dpp<ROR8>(t);
      p = pk(xr + t, 0.f);
      if (layer == 0) *(LAS u16*)(xb + (4 * xun + kg) * 2) = (u16)p;
      const bool ok = (or4(d0) & 0xffu) == 0u;
      RETRY(6, r0 = ld_sc1(rs, xoff));
    }
    if (layer == 0) {
      xr = bflo(p);
      X_SHARE(xb, 8, 14);
      X_FETCH(xb, 8);
      const float part = gemv128(a0, a1, Bq);
      const float p1 = dpp<0x55>(part), p2 = dpp<0xAA>(part), p3 = dpp<0xFF>(part);
      st_sc1(rs, qst, mkF(part, p1, p2, p3, T));
      if ((c & 7) == 0 && w == 0) STATS_PUBLISH(xb, 14, GO_ST2);
      STAMP(11);
      SLIP(3, 100);
    } else {
      if ((lane & 8) == 0) *(GAS u16*)(X.out + 128 * kq + 4 * xun + kg) = (u16)p;
      if (c == 0 && w == 0 && lane == 0) st1_sc1(rs, 0, X.E + 1u);
      STAMP(21);
    }
  }
}

__global__ void __launch_bounds__(512, 2)
fused_decode(const u16* x, const u16* kc, const u16* vc, const u16* wq, const u16* wo, const u16* wg, const u16* wu, const u16* wd, u16* out, unsigned char* ws) {
  extern __shared__ __attribute__((aligned(16))) unsigned char smem[];
  Ctx X;
  const int tid = threadIdx.x;
  X.lane = tid & 63; X.wv = __builtin_amdgcn_readfirstlane(tid >> 6); X.w = X.wv & 3; X.kg = X.lane >> 4; X.i16 = X.lane & 15;
  X.b = blockIdx.x; X.g = X.b & 7; X.c = X.b >> 3;
  X.t0 = rtc();
  unsigned pfv;
  {
    unsigned long long pc; unsigned len;
    asm volatile("s_getpc_b64 %0\n\tLpf_here:\n\ts_mov_b32 %1, Lpf_end - Lpf_here" : "=s"(pc), "=s"(len));
    unsigned off = (unsigned)(X.c * 8 + X.wv) * 128u; off = off < len ? off : 0u;
    pfv = *(const GAS unsigned*)(pc + off);
  }
  SCHED();
  asm volatile("" : "+s"(x), "+s"(kc), "+s"(vc), "+s"(wq), "+s"(wo), "+s"(wg), "+s"(wu), "+s"(wd), "+s"(out), "+s"(ws));
  SCHED();
  X.vc = vc; X.wo = wo; X.wg = wg; X.wu = wu; X.wd = wd; X.out = out; X.ws = ws;
  X.L = (LAS unsigned char*)smem;
  X.rs = __builtin_amdgcn_make_buffer_rsrc((void*)ws, 0, WS_BYTES, 0x00020000);
  const int lane = X.lane, w = X.w, kg = X.kg, i16 = X.i16, g = X.g, c = X.c, kq = c >> 3;
  const __amdgpu_buffer_rsrc_t rs = X.rs; LAS unsigned char* L = X.L;
  unsigned tch = 0;
  v4u Kf[8], Vf[8], Sw[10], wr[4], xf; v2u xa, xb; unsigned xu, E;
  if (X.wv >= 4) {
    DELAY(12);
    const size_t kvbase = ((size_t)g * SK + (SK - WIN) + 128 * c + 32 * w) * DH;
#pragma unroll
    for (int s = 0; s < 8; ++s) Kf[s] = ldg_nt(kc + kvbase + s * 512 + lane * 8);
#pragma unroll
    for (int j = 0; j < VPRE; ++j) Vf[j] = ldg_nt(vc + kvbase + (16 * (j >> 2) + 4 * kg + (j & 3)) * DH + 8 * i16);
  } else {
#pragma unroll
    for (int n = 0; n < 4; ++n) wr[n] = ldg_nt(wq + (size_t)(128 * kq + 32 * w + 8 * n + (lane >> 3)) * 4096 + 512 * g + 64 * (c & 7) + 8 * (lane & 7));
    xf = *(const GAS v4u*)(x + 8 * lane);
    xa = *(const GAS v2u*)(x + 4 * (32 * kq + 4 * kg + (i16 & 3))); xb = *(const GAS v2u*)(x + 4 * (32 * kq + 16 + 4 * kg + (i16 & 3)));
    xu = *(const GAS u16*)(x + 128 * kq + 4 * (8 * w + (lane & 7)) + kg);
    E = ld1_sc1(rs, 0);
  }
  SCHED();
  int wv2 = X.wv; asm volatile("" : "+s"(wv2));
  if (wv2 >= 4) run_A(X, Kf, Vf, Sw);
  int wv3 = X.wv; asm volatile("" : "+s"(wv3));
  if (wv3 < 4) {
#pragma unroll
    for (int n = 0; n < 4; ++n) *(LAS v4u*)(L + L_IMG + w * 4096 + n * 1024 + lane * 16) = wr[n];
    const unsigned hsh = (E + 1u) * 0x9E3779B1u;
    const unsigned T = __builtin_amdgcn_readfirstlane(((hsh & 0xbfffbf00u) | (((hsh >> 8) & 0xbfu) ^ 0xa5u)));
    X.E = E; X.T = T;
    LPOST(0);
    float xr = bflo(xu);
    v2u Bq[8], Bg[8]; v4u Aw[12], Bd[7];
    {
      float xv[8] = {bflo(xf.x), bfhi(xf.x), bflo(xf.y), bfhi(xf.y), bflo(xf.z), bfhi(xf.z), bflo(xf.w), bfhi(xf.w)};
      float s = 0.f;
#pragma unroll
      for (int k = 0; k < 8; ++k) s += xv[k];
      const float mean = xrow_sum(row_sum(s)) * (1.f / DM);
      float v = 0.f;
#pragma unroll
      for (int k = 0; k < 8; ++k) { const float d = xv[k] - mean; v = fmaf(d, d, v); }
      const float var = xrow_sum(row_sum(v)) * (1.f / DM);
      const float mb = rbf(mean), isd = 1.0f / rbf(sqrtf(rbf(rbf(var) + 1e-5f)));
      const float xi[8] = {bflo(xa.x), bfhi(xa.x), bflo(xa.y), bfhi(xa.y), bflo(xb.x), bfhi(xb.x), bflo(xb.y), bfhi(xb.y)};
      float xn[8];
#pragma unroll
      for (int k = 0; k < 8; ++k) xn[k] = rbf(xi[k] - mb) * isd;
      const v2u a0 = {pk(xn[0], xn[1]), pk(xn[2], xn[3])}, a1 = {pk(xn[4], xn[5]), pk(xn[6], xn[7])};
      LWAIT4(0);
#pragma unroll
      for (int u = 0; u < 8; ++u) Bq[u] = trread(L + L_IMG + (64 * (u >> 2) + 16 * kg + 4 * (u & 3) + (i16 >> 2)) * 128 + w * 32 + (i16 & 3) * 8);
      const int qo = (lane & 0x33) == 0 ? WS_Q0 + g * 8192 + kq * 2048 + ((c & 7) * 64 + w * 16 + i16) * 4 : OOB - 65536;
      {
        const float part = gemv128(a0, a1, Bq);
        const float p1 = dpp<0x55>(part), p2 = dpp<0xAA>(part), p3 = dpp<0xFF>(part);
        st_sc1(rs, qo, mkF(part, p1, p2, p3, T));
      }
      STAMP(1);
      {
        const v2u one = {ONES, ONES};
        const float part = gemv128(one, one, Bq);
        const float p1 = dpp<0x55>(part), p2 = dpp<0xAA>(part), p3 = dpp<0xFF>(part);
        st_sc1(rs, qo + 65536, mkF(part, p1, p2, p3, T));
      }
    }
    SCHED();
    {
      const v4u zz = {0u, 0u, 0u, 0u};
      const int slot = ((lane >> 4) & 1) * 4 + (lane & 3), rep = ((lane >> 2) & 3) + 4 * (lane >> 5), grp = WS_GRP + g * GRP_SZ;
      if (w == TW) {
        st_sc1(rs, WS_Y + rep * 32768 + (2 * g + (c & 1)) * 2048 + (c >> 1) * 128 + slot * 16, zz);
        st_sc1(rs, i16 < 8 ? WS_X + i16 * 16384 + g * 2048 + c * 64 + kg * 16 : OOB, zz);
      }
      st_sc1(rs, (c & 7) < 7 ? ((lane & 0x33) == 0 ? grp + GO_G + kq * 2048 + (w >> 1) * 1024 + (8 * (c & 7) + 4 * (w & 1) + (i16 >> 2)) * 16 : OOB)
                             : ((w == 0 && lane == 0) ? grp + GO_ST + kq * 16 : OOB), zz);
    }
    f32x4 csq = {0.f, 0.f, 0.f, 0.f}; float csg[2] = {0.f, 0.f};
    run_B<0>(X, Bq, Aw, Bg, Bd, xr, csq, csg);
    run_B<1>(X, Bq, Aw, Bg, Bd, xr, csq, csg);
  }
  asm volatile("" :: "v"(pfv), "v"(tch));
  STAMP_FLUSH();
  asm volatile("Lpf_end:");
}

extern "C" void kernel_launch(void* const* d_in, const int* in_sizes, int n_in, void* d_out, int out_size, void* d_ws, size_t ws_size,
                              hipStream_t stream) {
  (void)in_sizes; (void)n_in; (void)out_size; (void)ws_size;
  (void)hipFuncSetAttribute((const void*)fused_decode, hipFuncAttributeMaxDynamicSharedMemorySize, LDS_BYTES);
  hipLaunchKernelGGL(fused_decode, dim3(256), dim3(512), LDS_BYTES, stream, (const u16*)d_in[0], (const u16*)d_in[1], (const u16*)d_in[2],
                     (const u16*)d_in[3], (const u16*)d_in[4], (const u16*)d_in[6], (const u16*)d_in[7], (const u16*)d_in[8], (u16*)d_out,
                     (unsigned char*)d_ws);
}
```
